```python
import torch

def _softmax(x, axis=-1):
    return torch.softmax(x, dim=axis)
def _gelu(x):
    return torch.nn.functional.gelu(x, approximate="tanh")
def _silu(x):
    return torch.nn.functional.silu(x)
def _sigmoid(x):
    return torch.sigmoid(x)
def _layernorm(x, axis=-1, eps=1e-5):
    m = x.mean(dim=axis, keepdim=True)
    v = x.var(dim=axis, keepdim=True, unbiased=False)
    return (x - m) / torch.sqrt(v + eps)
def _scatter_add(tgt, idx, src, axis=0):
    return tgt.clone().index_add_(axis, idx, src)
def _segment_sum(src, seg, n):
    out = torch.zeros((n,) + src.shape[1:], dtype=src.dtype)
    return out.index_add_(0, seg, src)
def _pad(x, pw):
    flat = tuple(v for pair in reversed(pw) for v in pair)
    return torch.nn.functional.pad(x, flat)

kernel_name = 'parallel_sum__spectral_gfnet__glu_gated_geglu__prompt'

def setup_inputs():

    _w = (lambda a: (lambda t: t.to(torch.bfloat16) if t.is_floating_point() else t)(torch.as_tensor(a)))
    def _A_setup():
        B, S, D = 1, 2048, 1024
        Df = D // 2 + 1
        return {
            "x": _w((lambda *s: torch.randn(*s, dtype=torch.bfloat16) * 0.05)(B, S, D)),
            "w_r": _w((lambda *s: torch.randn(*s, dtype=torch.bfloat16) * 0.8)(S, Df) * 0.02),
            "w_i": _w((lambda *s: torch.randn(*s, dtype=torch.bfloat16) * 0.8)(S, Df) * 0.02),
        }
    _A_in = _A_setup()

    def _B_setup():
        B, S, Dm, Df = 1, 2048, 1024, 3584
        return {
            "x": _w((lambda *s: torch.randn(*s, dtype=torch.bfloat16) * 0.05)(B, S, Dm)),
            "w_gate": _w((lambda *s: torch.randn(*s, dtype=torch.bfloat16) * 0.4)(Dm, Df) * (Dm ** -0.5)),
            "w_up": _w((lambda *s: torch.randn(*s, dtype=torch.bfloat16) * 0.4)(Dm, Df) * (Dm ** -0.5)),
            "w_down": _w((lambda *s: torch.randn(*s, dtype=torch.bfloat16) * 0.4)(Df, Dm) * (Df ** -0.5)),
        }
    _B_in = _B_setup()

    return {
        "A_x": _A_in["x"],
        "A_w_r": _A_in["w_r"],
        "A_w_i": _A_in["w_i"],
        "B_x": _B_in["x"],
        "B_w_gate": _B_in["w_gate"],
        "B_w_up": _B_in["w_up"],
        "B_w_down": _B_in["w_down"],
    }

def reference(A_x, A_w_r, A_w_i, B_x, B_w_gate, B_w_up, B_w_down):
    def _A_ref(x, w_r, w_i):
        B, S, D = x.shape
        X = (lambda x, axes=(-2,-1): torch.fft.rfft2(x.float(), dim=axes))(x, axes=(-2, -1))
        W = w_r + 1j * w_i
        X = X * W
        y = (lambda x, s, axes=(-2,-1): torch.fft.irfft2(x, s=s, dim=axes).to(torch.bfloat16))(X, (S, D), axes=(-2, -1))
        return y

    def _B_ref(x, w_gate, w_up, w_down):
        g = torch.einsum("bsd,df->bsf", x, w_gate)
        u = torch.einsum("bsd,df->bsf", x, w_up)
        h = _gelu(g) * u
        return torch.einsum("bsf,fd->bsd", h, w_down)

    x = A_x
    xn = _layernorm(x, axis=-1)
    return x + _A_ref(xn, A_w_r, A_w_i) + _B_ref(xn, B_w_gate, B_w_up, B_w_down)
```

```cpp
#include <hip/hip_runtime.h>
#include <hip/hip_bf16.h>
#include <stdint.h>

#define LAS __attribute__((address_space(3)))
#define DI __device__ __forceinline__
typedef unsigned short u16;
typedef __bf16 bf16x8 __attribute__((ext_vector_type(8)));
typedef __bf16 bf16x4 __attribute__((ext_vector_type(4)));
typedef __bf16 bf16x2 __attribute__((ext_vector_type(2)));
typedef float f32x2 __attribute__((ext_vector_type(2)));
typedef float f32x4 __attribute__((ext_vector_type(4)));
typedef float f32x16 __attribute__((ext_vector_type(16)));
typedef unsigned u32x2 __attribute__((ext_vector_type(2)));
typedef unsigned u32x4 __attribute__((ext_vector_type(4)));
typedef unsigned u32x4u __attribute__((ext_vector_type(4), aligned(2)));

constexpr int S_ = 2048, D_ = 1024, F_ = 3584, DF_ = 513;
constexpr size_t WS_XN = 0;
constexpr size_t WS_H = 4u << 20;
constexpr size_t WS_RB = 20u << 20;
constexpr size_t WS_WB = 24u << 20;
constexpr size_t WS_Z = 29u << 20;
constexpr size_t WS_BASE = 34u << 20;

DI unsigned pack2(float a, float b) { f32x2 v = {a, b}; return __builtin_bit_cast(unsigned, __builtin_convertvector(v, bf16x2)); }
DI float bf_lo(unsigned u) { return __uint_as_float(u << 16); }
DI float bf_hi(unsigned u) { return __uint_as_float(u & 0xffff0000u); }
DI float rbf(float a) { return bf_lo(pack2(a, a)); }
DI int crow(int reg, int h) { return (reg & 3) + 8 * (reg >> 2) + 4 * h; }
DI int krow(int s, int h, int j) { return 16 * s + 8 * (j >> 2) + 4 * h + (j & 3); }
DI float cosr(float t) { return __builtin_amdgcn_cosf(t); }
DI float sinr(float t) { return __builtin_amdgcn_sinf(t); }
DI bf16x8 mk8(unsigned a, unsigned b, unsigned c, unsigned d) { u32x4 v = {a, b, c, d}; return __builtin_bit_cast(bf16x8, v); }
DI bf16x8 pack_step(const f32x16& x, int s) { return mk8(pack2(x[8 * s], x[8 * s + 1]), pack2(x[8 * s + 2], x[8 * s + 3]), pack2(x[8 * s + 4], x[8 * s + 5]), pack2(x[8 * s + 6], x[8 * s + 7])); }
DI bf16x4 tr_read(LAS const unsigned char* p) { return __builtin_amdgcn_ds_read_tr16_b64_v4bf16((LAS bf16x4*)p); }
DI bf16x8 cat4(bf16x4 a, bf16x4 b) { return __builtin_shufflevector(a, b, 0, 1, 2, 3, 4, 5, 6, 7); }
#define MFMA32(a, b, c) __builtin_amdgcn_mfma_f32_32x32x16_bf16((a), (b), (c), 0, 0, 0)
#define MFMA16(a, b, c) __builtin_amdgcn_mfma_f32_16x16x32_bf16((a), (b), (c), 0, 0, 0)
#define GLDS16(g, l) __builtin_amdgcn_global_load_lds((const unsigned*)(g), (LAS unsigned*)(l), 16, 0, 0)
#define WAIT_V(n) asm volatile("s_waitcnt vmcnt(" #n ")" ::: "memory")
#define WAIT_L(n) asm volatile("s_waitcnt lgkmcnt(" #n ")" ::: "memory")
#define BAR __builtin_amdgcn_s_barrier()
#define SCHED __builtin_amdgcn_sched_barrier(0)
DI f32x16 zero16() { f32x16 z; for (int i = 0; i < 16; ++i) z[i] = 0.f; return z; }
DI float wave_sum(float v) {
#pragma unroll
  for (int o = 32; o >= 1; o >>= 1) v += __shfl_xor(v, o);
  return v;
}

constexpr int K0_XT = 0;
constexpr int K0_RT = 16384;
constexpr int K0_WT = K0_RT + 8 * 2304;
constexpr int K0_LDS = K0_WT + 16 * 1056;
__global__ __launch_bounds__(512) void k0_ln_rowfft(const u16* __restrict__ x, const u16* __restrict__ w_r, const u16* __restrict__ w_i, unsigned char* __restrict__ ws) {
  __shared__ __attribute__((aligned(16))) unsigned char smem_[K0_LDS];
  LAS unsigned char* smem = (LAS unsigned char*)smem_;
  const int tid = threadIdx.x, lane = tid & 63, j = __builtin_amdgcn_readfirstlane(tid >> 6), w = blockIdx.x;
  const int q = w >> 6, s = (w & 63) + 64 * krow(q >> 1, q & 1, j);
  const int r = lane & 31, h = lane >> 5;
  const u32x4 x0 = *(const u32x4*)(x + (size_t)s * D_ + lane * 8), x1 = *(const u32x4*)(x + (size_t)s * D_ + 512 + lane * 8);
  const u32x4 wr = *(const u32x4u*)(w_r + (size_t)s * DF_ + lane * 8), wi = *(const u32x4u*)(w_i + (size_t)s * DF_ + lane * 8);
  u16 wn = 0;
  if (lane < 2) wn = (lane ? w_i : w_r)[(size_t)s * DF_ + 512];
  bf16x8 ec[2], ens[2], a1[2], a2[2];
  float twc[16], tws[16];
#pragma unroll
  for (int ks = 0; ks < 2; ++ks) {
    float c[8], n[8], p[8], m[8];
#pragma unroll
    for (int e = 0; e < 8; ++e) {
      const int a = krow(ks, h, e);
      const float t = (float)((a * r) & 31) * (1.f / 32.f);
      c[e] = cosr(t); n[e] = -sinr(t);
      const float t2 = (float)((a * (r >> 1)) & 31) * (1.f / 32.f);
      const float c2 = cosr(t2), s2 = sinr(t2);
      p[e] = (r & 1) ? -s2 : c2;
      m[e] = (r & 1) ? c2 : s2;
    }
    ec[ks] = mk8(pack2(c[0], c[1]), pack2(c[2], c[3]), pack2(c[4], c[5]), pack2(c[6], c[7]));
    ens[ks] = mk8(pack2(n[0], n[1]), pack2(n[2], n[3]), pack2(n[4], n[5]), pack2(n[6], n[7]));
    a1[ks] = mk8(pack2(p[0], p[1]), pack2(p[2], p[3]), pack2(p[4], p[5]), pack2(p[6], p[7]));
    a2[ks] = mk8(pack2(m[0], m[1]), pack2(m[2], m[3]), pack2(m[4], m[5]), pack2(m[6], m[7]));
  }
#pragma unroll
  for (int i = 0; i < 16; ++i) { const float t = (float)(crow(i, h) * r) * (1.f / 1024.f); twc[i] = cosr(t); tws[i] = sinr(t); }
  float v[16];
#pragma unroll
  for (int i = 0; i < 4; ++i) { v[2 * i] = bf_lo(x0[i]); v[2 * i + 1] = bf_hi(x0[i]); v[8 + 2 * i] = bf_lo(x1[i]); v[9 + 2 * i] = bf_hi(x1[i]); }
  float sum = 0.f;
#pragma unroll
  for (int i = 0; i < 16; ++i) sum += v[i];
  const float mean = wave_sum(sum) * (1.f / 1024.f);
  float sq = 0.f;
#pragma unroll
  for (int i = 0; i < 16; ++i) { const float d = v[i] - mean; sq += d * d; }
  const float var = wave_sum(sq) * (1.f / 1024.f);
  const float mb = rbf(mean), den = rbf(sqrtf(rbf(rbf(var) + 1e-5f)));
  const float rden = 1.0f / den;
  u32x4 n0, n1; float alt = 0.f;
#pragma unroll
  for (int i = 0; i < 4; ++i) {
    const unsigned d0 = pack2(v[2 * i] - mb, v[2 * i + 1] - mb), d1 = pack2(v[8 + 2 * i] - mb, v[9 + 2 * i] - mb);
    n0[i] = pack2(bf_lo(d0) * rden, bf_hi(d0) * rden); n1[i] = pack2(bf_lo(d1) * rden, bf_hi(d1) * rden);
    alt += (bf_lo(n0[i]) - bf_hi(n0[i])) + (bf_lo(n1[i]) - bf_hi(n1[i]));
  }
  u16* xn = (u16*)(ws + WS_XN);
  *(u32x4*)(xn + (size_t)s * D_ + lane * 8) = n0; *(u32x4*)(xn + (size_t)s * D_ + 512 + lane * 8) = n1;
  LAS unsigned char* xt = smem + K0_XT + j * 2048;
  *(LAS u32x4*)(xt + lane * 16) = n0; *(LAS u32x4*)(xt + 1024 + lane * 16) = n1;
  *(LAS u32x4*)(smem + K0_WT + j * 1056 + lane * 16) = wr; *(LAS u32x4*)(smem + K0_WT + (8 + j) * 1056 + lane * 16) = wi;
  const int pos32 = (((w >> 5) * 8 + (w & 7)) * 4 + ((w >> 3) & 3)) * 32;
  if (lane < 2) *(u16*)(ws + WS_WB + (size_t)512 * 8192 + pos32 + lane * 16 + j * 2) = wn;
  alt = wave_sum(alt);
  const int g2 = (lane >> 4) & 1, qq = (lane & 15) >> 2, pp = lane & 3;
  f32x16 tr = zero16(), ti = zero16();
#pragma unroll
  for (int ks = 0; ks < 2; ++ks) {
    const bf16x4 lo = tr_read(xt + (16 * ks + 4 * h + qq) * 64 + g2 * 32 + pp * 8), hi = tr_read(xt + (16 * ks + 8 + 4 * h + qq) * 64 + g2 * 32 + pp * 8);
    const bf16x8 xa = cat4(lo, hi);
    tr = MFMA32(xa, ec[ks], tr); ti = MFMA32(xa, ens[ks], ti);
  }
  f32x16 ur, ui;
#pragma unroll
  for (int i = 0; i < 16; ++i) { ur[i] = tr[i] * twc[i] + ti[i] * tws[i]; ui[i] = ti[i] * twc[i] - tr[i] * tws[i]; }
  f32x16 o = zero16();
#pragma unroll
  for (int ks = 0; ks < 2; ++ks) { o = MFMA32(a1[ks], pack_step(ur, ks), o); o = MFMA32(a2[ks], pack_step(ui, ks), o); }
  if (lane == 0) o[1] = alt;
#pragma unroll
  for (int g = 0; g < 4; ++g) { u32x2 pv = {pack2(o[4 * g], o[4 * g + 1]), pack2(o[4 * g + 2], o[4 * g + 3])}; *(LAS u32x2*)(smem + K0_RT + j * 2304 + r * 72 + (8 * g + 4 * h) * 2) = pv; }
  __syncthreads();
  const int i16 = lane & 15, g4 = lane >> 4;
#pragma unroll
  for (int it = 0; it < 2; ++it) {
    const int base = j * 128 + it * 64 + g4 * 16;
    const int src = base + pp * 4;
    LAS const unsigned char* p = smem + K0_RT + qq * 2304 + (src >> 5) * 72 + (src & 31) * 2;
    const bf16x8 c = cat4(tr_read(p), tr_read(p + 4 * 2304));
    const int ch = base + i16;
    *(bf16x8*)(ws + WS_RB + (size_t)(ch >> 1) * 8192 + pos32 + (ch & 1) * 16) = c;
  }
#pragma unroll
  for (int it = 0; it < 2; ++it) {
    const int ka0 = j * 4, kbh = it * 8;
    const int us = g4 * 4 + pp;
    LAS const unsigned char* p = smem + K0_WT + ((us & 1) * 8 + qq) * 1056 + (ka0 + 32 * (kbh + (us >> 1))) * 2;
    const bf16x8 c = cat4(tr_read(p), tr_read(p + 4 * 1056));
    const int ur_ = g4 * 4 + (i16 >> 2), ka = ka0 + (i16 & 3);
    *(bf16x8*)(ws + WS_WB + (size_t)(ka * 16 + kbh + (ur_ >> 1)) * 8192 + pos32 + (ur_ & 1) * 16) = c;
  }
}

struct CF { bf16x8 c[2], s[2]; };
DI void mkcf(CF& f, int mode, int r, int h) {
#pragma unroll
  for (int ks = 0; ks < 2; ++ks) {
    float c[8], s[8];
#pragma unroll
    for (int e = 0; e < 8; ++e) {
      const int a = krow(ks, h, e);
      const int num = mode == 0 ? 2 * a * r : (mode == 1 ? (2 * a + 1) * r : (2 * r + 1) * a);
      const float t = (float)(num & 63) * (1.f / 64.f);
      c[e] = cosr(t); s[e] = sinr(t);
    }
    f.c[ks] = mk8(pack2(c[0], c[1]), pack2(c[2], c[3]), pack2(c[4], c[5]), pack2(c[6], c[7]));
    f.s[ks] = mk8(pack2(s[0], s[1]), pack2(s[2], s[3]), pack2(s[4], s[5]), pack2(s[6], s[7]));
  }
}
DI bf16x8 neg8(bf16x8 v) { u32x4 u = __builtin_bit_cast(u32x4, v); u ^= 0x80008000u; return __builtin_bit_cast(bf16x8, u); }
template <bool DA, bool INV>
DI void cmm(f32x16& re, f32x16& im, const bf16x8 (&xr)[2], const bf16x8 (&xi)[2], const CF& f) {
  re = zero16(); im = zero16();
#pragma unroll
  for (int ks = 0; ks < 2; ++ks) {
    const bf16x8 p = INV ? neg8(xi[ks]) : xi[ks], m = INV ? xr[ks] : neg8(xr[ks]);
    if (DA) { re = MFMA32(xr[ks], f.c[ks], re); im = MFMA32(xi[ks], f.c[ks], im); re = MFMA32(p, f.s[ks], re); im = MFMA32(m, f.s[ks], im); }
    else    { re = MFMA32(f.c[ks], xr[ks], re); im = MFMA32(f.c[ks], xi[ks], im); re = MFMA32(f.s[ks], p, re); im = MFMA32(f.s[ks], m, im); }
  }
}
#define PACK4() do { _Pragma("unroll") for (int ks = 0; ks < 2; ++ks) { pr[0][ks] = pack_step(ar, ks); pi[0][ks] = pack_step(ai, ks); pr[1][ks] = pack_step(br, ks); pi[1][ks] = pack_step(bi, ks); } } while (0)

DI void col_pass(unsigned char* __restrict__ ws, int col0) {
  const int lane = threadIdx.x & 63, r = lane & 31, h = lane >> 5;
  CF e0, e1, e1t; float twc[16], tws[16];
  mkcf(e0, 0, r, h); mkcf(e1, 1, r, h); mkcf(e1t, 2, r, h);
#pragma unroll
  for (int i = 0; i < 16; ++i) { const float t = (float)(crow(i, h) * r) * (1.f / 2048.f); twc[i] = cosr(t) * (1.f / 1024.f); tws[i] = sinr(t) * (1.f / 1024.f); }
  const int lo = (h * 64 + (r & 7) * 4 + (r >> 3)) * 32;
  const int nit = col0 == 0 ? 3 : 2;
#pragma unroll 1
  for (int it = 0; it < nit; ++it) {
    const int col = it == 2 ? 0 : col0 + it, wcol = it == 2 ? 512 : col;
    const bool real = col == 0;
    const unsigned char* rp = ws + WS_RB + (size_t)col * 8192 + lo + (it == 2 ? 16 : 0);
    const unsigned char* wp = ws + WS_WB + (size_t)wcol * 8192 + lo;
    bf16x8 pr[2][2], pi[2][2]; u32x4 wr[2][2], wi[2][2];
#pragma unroll
    for (int b = 0; b < 2; ++b)
#pragma unroll
      for (int ks = 0; ks < 2; ++ks) {
        pr[b][ks] = *(const bf16x8*)(rp + ks * 4096 + b * 1024);
        pi[b][ks] = real ? mk8(0, 0, 0, 0) : *(const bf16x8*)(rp + ks * 4096 + b * 1024 + 16);
      }
#pragma unroll
    for (int b = 0; b < 2; ++b)
#pragma unroll
      for (int ks = 0; ks < 2; ++ks) { wr[b][ks] = *(const u32x4*)(wp + ks * 4096 + b * 1024); wi[b][ks] = *(const u32x4*)(wp + ks * 4096 + b * 1024 + 16); }
    f32x16 ar, ai, br, bi;
    cmm<true, false>(ar, ai, pr[0], pi[0], e0);
    cmm<true, false>(br, bi, pr[1], pi[1], e1);
#pragma unroll
    for (int i = 0; i < 16; ++i) {
      const float sr = ar[i] + br[i], si = ai[i] + bi[i], dr = ar[i] - br[i], di = ai[i] - bi[i];
      ar[i] = sr * twc[i] + si * tws[i]; ai[i] = si * twc[i] - sr * tws[i];
      br[i] = dr * twc[i] + di * tws[i]; bi[i] = di * twc[i] - dr * tws[i];
    }
    PACK4();
    cmm<false, false>(ar, ai, pr[0], pi[0], e0);
    cmm<false, false>(br, bi, pr[1], pi[1], e1t);
#pragma unroll
    for (int i = 0; i < 16; ++i) {
      const unsigned u0r = wr[0][i >> 3][(i >> 1) & 3], u0i = wi[0][i >> 3][(i >> 1) & 3], u1r = wr[1][i >> 3][(i >> 1) & 3], u1i = wi[1][i >> 3][(i >> 1) & 3];
      const float w0r = (i & 1) ? bf_hi(u0r) : bf_lo(u0r), w0i = (i & 1) ? bf_hi(u0i) : bf_lo(u0i), w1r = (i & 1) ? bf_hi(u1r) : bf_lo(u1r), w1i = (i & 1) ? bf_hi(u1i) : bf_lo(u1i);
      const float t0 = ar[i], t1 = br[i];
      ar[i] = t0 * w0r - ai[i] * w0i; ai[i] = t0 * w0i + ai[i] * w0r;
      br[i] = t1 * w1r - bi[i] * w1i; bi[i] = t1 * w1i + bi[i] * w1r;
    }
    PACK4();
    cmm<true, true>(ar, ai, pr[0], pi[0], e0);
    cmm<true, true>(br, bi, pr[1], pi[1], e1);
#pragma unroll
    for (int i = 0; i < 16; ++i) {
      const float sr = ar[i] + br[i], si = ai[i] + bi[i], dr = ar[i] - br[i], di = ai[i] - bi[i];
      ar[i] = sr * twc[i] - si * tws[i]; ai[i] = si * twc[i] + sr * tws[i];
      br[i] = dr * twc[i] - di * tws[i]; bi[i] = di * twc[i] + dr * tws[i];
    }
    PACK4();
    cmm<false, true>(ar, ai, pr[0], pi[0], e0);
    cmm<false, true>(br, bi, pr[1], pi[1], e1t);
    const float sc = real ? 0.5f : 1.f;
    unsigned char* zp = ws + WS_Z + (size_t)wcol * 8192 + lane * 16;
#pragma unroll
    for (int g = 0; g < 4; ++g) {
      u32x4 o0, o1;
#pragma unroll
      for (int n = 0; n < 4; ++n) { o0[n] = pack2(ar[4 * g + n] * sc, ai[4 * g + n]); o1[n] = pack2(br[4 * g + n] * sc, bi[4 * g + n]); }
      *(u32x4*)(zp + g * 2048) = o0; *(u32x4*)(zp + g * 2048 + 1024) = o1;
    }
  }
}
__global__ __launch_bounds__(512, 2) void k_col(unsigned char* __restrict__ ws) {
  const int wv = __builtin_amdgcn_readfirstlane(threadIdx.x >> 6);
  col_pass(ws, blockIdx.x * 16 + wv * 2);
}

DI void row_inv(LAS unsigned char* smem, const u16* __restrict__ x, unsigned char* __restrict__ ws, int c) {
  const int lane = threadIdx.x & 63, r = lane & 31, h = lane >> 5, wv = __builtin_amdgcn_readfirstlane(threadIdx.x >> 6);
#pragma unroll
  for (int i = 0; i < 16; ++i) {
    const int col = (wv * 16 + i) * 4 + (lane >> 4);
    GLDS16(ws + WS_Z + (size_t)col * 8192 + c * 256 + (((lane & 15) ^ ((col >> 4) & 15)) * 16), smem + (wv * 16 + i) * 1024);
  }
  const int g = c >> 3, bz = (c >> 2) & 1, hz = (c >> 1) & 1;
  u32x4 xx[2][4][2]; u32x4 nyq[2];
#pragma unroll
  for (int o = 0; o < 2; ++o) {
    const int ro = wv + 8 * o;
    nyq[o] = *(const u32x4*)(ws + WS_Z + (size_t)512 * 8192 + c * 256 + ro * 16);
#pragma unroll
    for (int n = 0; n < 4; ++n) {
      const int tok = 64 * (8 * g + 4 * hz + n) + 32 * bz + (c & 1) * 16 + ro;
      const u16* px = x + (size_t)tok * D_ + r * 32 + h * 16;
      xx[o][n][0] = *(const u32x4*)px; xx[o][n][1] = *(const u32x4*)(px + 8);
    }
  }
  const int b = 16 * ((r >> 2) & 1) + (r & 3) + 4 * (r >> 3);
  bf16x8 b1[2], b2[2], ec[2], ens[2]; float twc[16], tws[16];
#pragma unroll
  for (int ks = 0; ks < 2; ++ks) {
    float p[8], m[8], cc[8], nn[8];
#pragma unroll
    for (int e = 0; e < 8; ++e) {
      const int kb = 8 * h + 4 * ks + (e >> 1);
      const float t = (float)((kb * b) & 31) * (1.f / 32.f);
      const float c1 = cosr(t), s1 = sinr(t);
      p[e] = (e & 1) ? -s1 : c1; m[e] = (e & 1) ? c1 : s1;
      const float t2 = (float)((krow(ks, h, e) * r) & 31) * (1.f / 32.f);
      cc[e] = cosr(t2); nn[e] = -sinr(t2);
    }
    b1[ks] = mk8(pack2(p[0], p[1]), pack2(p[2], p[3]), pack2(p[4], p[5]), pack2(p[6], p[7]));
    b2[ks] = mk8(pack2(m[0], m[1]), pack2(m[2], m[3]), pack2(m[4], m[5]), pack2(m[6], m[7]));
    ec[ks] = mk8(pack2(cc[0], cc[1]), pack2(cc[2], cc[3]), pack2(cc[4], cc[5]), pack2(cc[6], cc[7]));
    ens[ks] = mk8(pack2(nn[0], nn[1]), pack2(nn[2], nn[3]), pack2(nn[4], nn[5]), pack2(nn[6], nn[7]));
  }
#pragma unroll
  for (int i = 0; i < 16; ++i) { const float t = (float)(crow(i, h) * b) * (1.f / 1024.f); twc[i] = cosr(t); tws[i] = sinr(t); }
  WAIT_V(0); BAR;
#pragma unroll
  for (int o = 0; o < 2; ++o) {
    const int ro = wv + 8 * o;
    u32x4 z[2][4];
#pragma unroll
    for (int ks = 0; ks < 2; ++ks)
#pragma unroll
      for (int u = 0; u < 4; ++u) z[ks][u] = *(LAS const u32x4*)(smem + (r * 16 + 8 * h + 4 * ks + u) * 256 + ((ro ^ (r & 15)) * 16));
#pragma unroll
    for (int n = 0; n < 4; ++n) {
      f32x16 ur = zero16(), ui = zero16();
#pragma unroll
      for (int ks = 0; ks < 2; ++ks) { const bf16x8 za = mk8(z[ks][0][n], z[ks][1][n], z[ks][2][n], z[ks][3][n]); ur = MFMA32(za, b1[ks], ur); ui = MFMA32(za, b2[ks], ui); }
      const float ny = bf_lo(nyq[o][n]);
      f32x16 vr, vi;
#pragma unroll
      for (int i = 0; i < 16; ++i) { vr[i] = ur[i] * twc[i] - ui[i] * tws[i]; vi[i] = ui[i] * twc[i] + ur[i] * tws[i]; }
      f32x16 y;
#pragma unroll
      for (int i = 0; i < 16; ++i) y[i] = (i & 1) ? -ny : ny;
#pragma unroll
      for (int ks = 0; ks < 2; ++ks) { y = MFMA32(pack_step(vr, ks), ec[ks], y); y = MFMA32(pack_step(vi, ks), ens[ks], y); }
      u32x4 ov[2];
#pragma unroll
      for (int i = 0; i < 8; ++i) {
        const unsigned yb = pack2(y[2 * i], y[2 * i + 1]), xb = xx[o][n][i >> 2][i & 3];
        ov[i >> 2][i & 3] = pack2(bf_lo(xb) + bf_lo(yb), bf_hi(xb) + bf_hi(yb));
      }
      const int tok = 64 * (8 * g + 4 * hz + n) + 32 * bz + (c & 1) * 16 + ro;
      u16* po = (u16*)(ws + WS_BASE) + (size_t)tok * D_ + r * 32 + h * 16;
      *(u32x4*)po = ov[0]; *(u32x4*)(po + 8) = ov[1];
    }
  }
}
__global__ __launch_bounds__(512, 2) void k_rowinv(const u16* __restrict__ x, unsigned char* __restrict__ ws) {
  extern __shared__ __attribute__((aligned(16))) unsigned char smem_[];
  row_inv((LAS unsigned char*)smem_, x, ws, blockIdx.x);
}

constexpr int HTB = 16384;
DI float geglu(float g, float u) {
  const float k1 = -2.f * 0.7978845608028654f * 1.4426950408889634f, k2 = k1 * 0.044715f;
  const float e = __builtin_amdgcn_exp2f(g * (k1 + k2 * g * g));
  return g * u * __builtin_amdgcn_rcpf(1.f + e);
}
DI void gemm1_tile(LAS unsigned char* lds, const u16* __restrict__ xn, const u16* __restrict__ wg, const u16* __restrict__ wu, unsigned char* __restrict__ hbuf, int pm, int pn) {
  const int tid = threadIdx.x, wid = __builtin_amdgcn_readfirstlane(tid >> 6), lane = tid & 63, wr = wid >> 2, wc = wid & 3, fr = lane & 15, fq = lane >> 4;
  unsigned voffA[2], voffB[2];
#pragma unroll
  for (int i = 0; i < 2; ++i) {
    const int b = tid * 16 + i * 8192;
    const int ra = b >> 7, ca = ((b >> 4) & 7) ^ ((ra >> 1) & 7);
    voffA[i] = (unsigned)(ra * D_ + ca * 8) * 2u;
    const int kb = b >> 8, p = (b >> 4) & 15, u = (p >> 1) ^ ((kb & 3) | (((kb >> 3) & 1) << 2));
    voffB[i] = (unsigned)(kb * F_ + u * 16 + (p & 1) * 8) * 2u;
  }
  const unsigned ldsw = (unsigned)wid * 1024u;
  const int aoff0 = (wr * 64 + fr) * 128 + ((fq ^ (fr >> 1)) * 16), aoff1 = aoff0 ^ 64;
  const int q4 = (lane & 15) >> 2, pp = lane & 3;
  const int boff0 = (8 * fq + q4) * 256 + (((wc * 2) ^ (q4 | ((fq & 1) << 2))) * 32) + pp * 8, boff1 = boff0 ^ 32;
  const char* cA = (const char*)xn + (size_t)pm * 256 * D_ * 2;
  const char* cG = (const char*)wg + (size_t)pn * 128 * 2;
  const char* cU = (const char*)wu + (size_t)pn * 128 * 2;
  constexpr size_t hA = (size_t)128 * D_ * 2, kA = 128, kB = (size_t)64 * F_ * 2;
#define SA(b, h) (((b) * 2 + (h)) * HTB)
#define SB(b, h) ((4 + (b) * 2 + (h)) * HTB)
#define STAGE(bufoff, gbase, voff) do { _Pragma("unroll") for (int _i = 0; _i < 2; ++_i) GLDS16((const char*)(gbase) + (voff)[_i], lds + (bufoff) + ldsw + _i * 8192); } while (0)
#define LDA(dst, b, h) do { _Pragma("unroll") for (int m = 0; m < 4; ++m) { dst[m][0] = *(const LAS bf16x8*)(lds + SA(b, h) + aoff0 + m * 2048); dst[m][1] = *(const LAS bf16x8*)(lds + SA(b, h) + aoff1 + m * 2048); } } while (0)
#define LDB(dst, b, h) do { _Pragma("unroll") for (int k = 0; k < 2; ++k) { \
    dst[0][k] = cat4(tr_read(lds + SB(b, h) + boff0 + k * 8192), tr_read(lds + SB(b, h) + boff0 + k * 8192 + 1024)); \
    dst[1][k] = cat4(tr_read(lds + SB(b, h) + boff1 + k * 8192), tr_read(lds + SB(b, h) + boff1 + k * 8192 + 1024)); } } while (0)
#define MMA(ai, bj, At, Bt) do { __builtin_amdgcn_s_setprio(1); _Pragma("unroll") for (int m = 0; m < 4; ++m) _Pragma("unroll") for (int n = 0; n < 2; ++n) _Pragma("unroll") for (int k = 0; k < 2; ++k) \
    acc[ai][bj][m][n] = MFMA16(Bt[n][k], At[m][k], acc[ai][bj][m][n]); __builtin_amdgcn_s_setprio(0); } while (0)
  f32x4 acc[2][2][4][2];
#pragma unroll
  for (int a = 0; a < 2; ++a)
#pragma unroll
    for (int b = 0; b < 2; ++b)
#pragma unroll
      for (int m = 0; m < 4; ++m)
#pragma unroll
        for (int n = 0; n < 2; ++n) acc[a][b][m][n] = (f32x4){0.f, 0.f, 0.f, 0.f};
  bf16x8 At[4][2], B0[2][2], B1[2][2];
  constexpr int nt = D_ / 64;
  STAGE(SB(0, 0), cG, voffB); STAGE(SA(0, 0), cA, voffA); STAGE(SB(0, 1), cU, voffB); STAGE(SA(0, 1), cA + hA, voffA);
  if (wr == 1) BAR;
  WAIT_V(4); BAR;
  STAGE(SB(1, 0), cG + kB, voffB); STAGE(SA(1, 0), cA + kA, voffA); STAGE(SB(1, 1), cU + kB, voffB);
  WAIT_V(6); BAR;
  for (int t = 0; t < nt - 2; t += 2) {
    const char* a1 = cA + (size_t)(t + 1) * kA; const char* a2 = a1 + kA; const char* a3 = a2 + kA;
    const char* g2 = cG + (size_t)(t + 2) * kB; const char* g3 = g2 + kB; const char* u2 = cU + (size_t)(t + 2) * kB; const char* u3 = u2 + kB;
    LDB(B0, 0, 0); SCHED; LDA(At, 0, 0); STAGE(SA(1, 1), a1 + hA, voffA);
    WAIT_L(8); BAR; WAIT_L(0); MMA(0, 0, At, B0); BAR; SCHED;
    LDB(B1, 0, 1); STAGE(SB(0, 0), g2, voffB);
    BAR; WAIT_L(0); MMA(0, 1, At, B1); BAR;
    LDA(At, 0, 1); STAGE(SA(0, 0), a2, voffA);
    BAR; WAIT_L(0); MMA(1, 0, At, B0); BAR; SCHED;
    STAGE(SB(0, 1), u2, voffB);
    WAIT_V(6); BAR; MMA(1, 1, At, B1); BAR;
    LDB(B0, 1, 0); SCHED; LDA(At, 1, 0); STAGE(SA(0, 1), a2 + hA, voffA);
    WAIT_L(8); BAR; WAIT_L(0); MMA(0, 0, At, B0); BAR; SCHED;
    LDB(B1, 1, 1); STAGE(SB(1, 0), g3, voffB);
    BAR; WAIT_L(0); MMA(0, 1, At, B1); BAR;
    LDA(At, 1, 1); STAGE(SA(1, 0), a3, voffA);
    BAR; WAIT_L(0); MMA(1, 0, At, B0); BAR; SCHED;
    STAGE(SB(1, 1), u3, voffB);
    WAIT_V(6); BAR; MMA(1, 1, At, B1); BAR;
  }
  { LDB(B0, 0, 0); WAIT_V(0); LDA(At, 0, 0); STAGE(SA(1, 1), cA + (size_t)(nt - 1) * kA + hA, voffA);
    BAR; WAIT_L(0); MMA(0, 0, At, B0); BAR;
    LDB(B1, 0, 1); BAR; WAIT_L(0); MMA(0, 1, At, B1); BAR;
    LDA(At, 0, 1); BAR; WAIT_L(0); MMA(1, 0, At, B0); MMA(1, 1, At, B1); BAR; }
  { LDB(B0, 1, 0); LDA(At, 1, 0); BAR; WAIT_L(0); MMA(0, 0, At, B0); BAR;
    LDB(B1, 1, 1); WAIT_V(0); BAR; WAIT_L(0); MMA(0, 1, At, B1); BAR;
    LDA(At, 1, 1); BAR; WAIT_L(0); MMA(1, 0, At, B0); MMA(1, 1, At, B1); BAR; }
  if (wr == 0) BAR;
#pragma unroll
  for (int ai = 0; ai < 2; ++ai)
#pragma unroll
    for (int m = 0; m < 4; ++m) {
      u32x4 o;
#pragma unroll
      for (int n = 0; n < 2; ++n) {
        const f32x4 g = acc[ai][0][m][n], u = acc[ai][1][m][n];
        o[2 * n] = pack2(geglu(g[0], u[0]), geglu(g[1], u[1])); o[2 * n + 1] = pack2(geglu(g[2], u[2]), geglu(g[3], u[3]));
      }
      *(u32x4*)(hbuf + ((size_t)((pm * 2 + ai) * 112 + pn * 4 + wc) * 8 + wr * 4 + m) * 1024 + lane * 16) = o;
    }
#undef SA
#undef SB
#undef STAGE
#undef LDA
#undef LDB
#undef MMA
}
__global__ __launch_bounds__(512, 2) void k1_gemm1(const u16* __restrict__ wg, const u16* __restrict__ wu, unsigned char* __restrict__ ws) {
  extern __shared__ __attribute__((aligned(16))) unsigned char smem_[];
  const int xcd = blockIdx.x & 7, idx = blockIdx.x >> 3;
  if (idx >= 28) return;
  const int pm = (xcd & 1) * 4 + (idx & 3), pn = (xcd >> 1) * 7 + (idx >> 2);
  gemm1_tile((LAS unsigned char*)smem_, (const u16*)(ws + WS_XN), wg, wu, ws + WS_H, pm, pn);
}

constexpr int G2_SLOT = 24576, G2_NS = 6, G2_NT = F_ / 64;
__global__ __launch_bounds__(512, 2) void k2_gemm2(const u16* __restrict__ wd, const unsigned char* __restrict__ ws, u16* __restrict__ out) {
  extern __shared__ __attribute__((aligned(16))) unsigned char smem_[];
  LAS unsigned char* lds = (LAS unsigned char*)smem_;
  const int tid = threadIdx.x, wid = __builtin_amdgcn_readfirstlane(tid >> 6), lane = tid & 63;
  const int xcd = blockIdx.x & 7, idx = blockIdx.x >> 3;
  const int tm = (xcd >> 1) * 4 + (idx & 3), tn = (xcd & 1) * 8 + (idx >> 2);
  const int kg = wid >> 2, wm = (wid >> 1) & 1, wn = wid & 1, fq = lane >> 4, fr = lane & 15, q4 = (lane & 15) >> 2, pp = lane & 3;
  const unsigned char* hsrc = ws + WS_H + (size_t)tm * 112 * 8192 + tid * 16;
  const int wrow = tid >> 3, wp = tid & 7;
  const unsigned char* wsrc = (const unsigned char*)wd + ((size_t)wrow * D_ + tn * 64 + (((wp >> 1) ^ ((wrow >> 1) & 3)) * 16) + (wp & 1) * 8) * 2;
  const unsigned ldsw = wid * 1024u;
#define STAGE2(t, slot) do { GLDS16(hsrc + (size_t)(t) * 16384, lds + (slot) * G2_SLOT + ldsw); GLDS16(hsrc + (size_t)(t) * 16384 + 8192, lds + (slot) * G2_SLOT + 8192 + ldsw); \
    GLDS16(wsrc + (size_t)(t) * (64 * D_ * 2), lds + (slot) * G2_SLOT + 16384 + ldsw); } while (0)
  const int hoff = (kg * 8 + wm * 4) * 1024 + lane * 16;
  const int woff0 = 16384 + (kg * 32 + 4 * fq + q4) * 128 + (((wn * 2) ^ ((2 * fq + (q4 >> 1)) & 3)) * 32) + pp * 8, woff1 = woff0 ^ 32;
#define LD2(H, W, slot) do { _Pragma("unroll") for (int m = 0; m < 4; ++m) H[m] = *(const LAS bf16x8*)(lds + (slot) * G2_SLOT + hoff + m * 1024); \
    W[0] = cat4(tr_read(lds + (slot) * G2_SLOT + woff0), tr_read(lds + (slot) * G2_SLOT + woff0 + 2048)); \
    W[1] = cat4(tr_read(lds + (slot) * G2_SLOT + woff1), tr_read(lds + (slot) * G2_SLOT + woff1 + 2048)); } while (0)
#define MM2(H, W) do { _Pragma("unroll") for (int m = 0; m < 4; ++m) _Pragma("unroll") for (int n = 0; n < 2; ++n) acc[m][n] = MFMA16(W[n], H[m], acc[m][n]); } while (0)
  f32x4 acc[4][2];
#pragma unroll
  for (int m = 0; m < 4; ++m) { acc[m][0] = (f32x4){0.f, 0.f, 0.f, 0.f}; acc[m][1] = (f32x4){0.f, 0.f, 0.f, 0.f}; }
  bf16x8 Ha[4], Wa[2], Hb[4], Wb[2];
  STAGE2(0, 0); STAGE2(1, 1); STAGE2(2, 2); STAGE2(3, 3); STAGE2(4, 4); STAGE2(5, 5);
  WAIT_V(15); BAR;
  LD2(Ha, Wa, 0);
#define ITER(t, slot, nslot, Hc, Wc, Hn, Wn, VW, DOSTAGE) do { WAIT_L(0); WAIT_V(VW); BAR; LD2(Hn, Wn, nslot); if (DOSTAGE) STAGE2((t) + 6, slot); SCHED; MM2(Hc, Wc); SCHED; } while (0)
  for (int t = 0; t < 48; t += 6) {
    ITER(t + 0, 0, 1, Ha, Wa, Hb, Wb, 12, true);
    ITER(t + 1, 1, 2, Hb, Wb, Ha, Wa, 12, true);
    ITER(t + 2, 2, 3, Ha, Wa, Hb, Wb, 12, true);
    ITER(t + 3, 3, 4, Hb, Wb, Ha, Wa, 12, true);
    ITER(t + 4, 4, 5, Ha, Wa, Hb, Wb, 12, true);
    ITER(t + 5, 5, 0, Hb, Wb, Ha, Wa, 12, true);
  }
  ITER(48, 0, 1, Ha, Wa, Hb, Wb, 12, true);
  ITER(49, 1, 2, Hb, Wb, Ha, Wa, 12, true);
  ITER(50, 2, 3, Ha, Wa, Hb, Wb, 12, false);
  ITER(51, 3, 4, Hb, Wb, Ha, Wa, 9, false);
  ITER(52, 4, 5, Ha, Wa, Hb, Wb, 6, false);
  ITER(53, 5, 0, Hb, Wb, Ha, Wa, 3, false);
  ITER(54, 0, 1, Ha, Wa, Hb, Wb, 0, false);
  WAIT_L(0); SCHED; MM2(Hb, Wb);
  const int tok = tid >> 2, dq = tid & 3;
  const size_t go = (size_t)(tm * 128 + tok) * D_ + tn * 64 + dq * 16;
  const u32x4 b0 = *(const u32x4*)((const u16*)(ws + WS_BASE) + go), b1 = *(const u32x4*)((const u16*)(ws + WS_BASE) + go + 8);
  __syncthreads();
#pragma unroll
  for (int m = 0; m < 4; ++m)
#pragma unroll
    for (int n = 0; n < 2; ++n) *(LAS f32x4*)(lds + (kg * 128 + wm * 64 + m * 16 + fr) * 272 + (wn * 32 + n * 16 + fq * 4) * 4) = acc[m][n];
  __syncthreads();
  u32x4 o[2];
#pragma unroll
  for (int i = 0; i < 4; ++i) {
    const f32x4 a = *(const LAS f32x4*)(lds + tok * 272 + (dq * 16 + i * 4) * 4) + *(const LAS f32x4*)(lds + (128 + tok) * 272 + (dq * 16 + i * 4) * 4);
    const unsigned p0 = pack2(a[0], a[1]), p1 = pack2(a[2], a[3]);
    const unsigned c0 = (i < 2 ? b0 : b1)[(2 * i) & 3], c1 = (i < 2 ? b0 : b1)[(2 * i + 1) & 3];
    o[i >> 1][(2 * i) & 3] = pack2(bf_lo(c0) + bf_lo(p0), bf_hi(c0) + bf_hi(p0));
    o[i >> 1][(2 * i + 1) & 3] = pack2(bf_lo(c1) + bf_lo(p1), bf_hi(c1) + bf_hi(p1));
  }
  *(u32x4*)(out + go) = o[0]; *(u32x4*)(out + go + 8) = o[1];
}

extern "C" void kernel_launch(void* const* d_in, const int* in_sizes, int n_in, void* d_out, int out_size, void* d_ws, size_t ws_size, hipStream_t stream) {
  const u16* x = (const u16*)d_in[0]; const u16* w_r = (const u16*)d_in[1]; const u16* w_i = (const u16*)d_in[2];
  const u16* wg = (const u16*)d_in[4]; const u16* wu = (const u16*)d_in[5]; const u16* wd = (const u16*)d_in[6];
  unsigned char* ws = (unsigned char*)d_ws;
  (void)hipFuncSetAttribute((const void*)k_rowinv, hipFuncAttributeMaxDynamicSharedMemorySize, 131072);
  (void)hipFuncSetAttribute((const void*)k1_gemm1, hipFuncAttributeMaxDynamicSharedMemorySize, 131072);
  (void)hipFuncSetAttribute((const void*)k2_gemm2, hipFuncAttributeMaxDynamicSharedMemorySize, G2_SLOT * G2_NS);
  k0_ln_rowfft<<<256, 512, 0, stream>>>(x, w_r, w_i, ws);
  k_col<<<32, 512, 0, stream>>>(ws);
  k_rowinv<<<32, 512, 131072, stream>>>(x, ws);
  k1_gemm1<<<256, 512, 131072, stream>>>(wg, wu, ws);
  k2_gemm2<<<256, 512, G2_SLOT * G2_NS, stream>>>(wd, ws, (u16*)d_out);
}
```

```cpp
#include <hip/hip_runtime.h>
#include <hip/hip_bf16.h>
#include <stdint.h>

#define LAS __attribute__((address_space(3)))
#define DI __device__ __forceinline__
typedef unsigned short u16;
typedef __bf16 bf16x8 __attribute__((ext_vector_type(8)));
typedef __bf16 bf16x4 __attribute__((ext_vector_type(4)));
typedef __bf16 bf16x2 __attribute__((ext_vector_type(2)));
typedef float f32x2 __attribute__((ext_vector_type(2)));
typedef float f32x4 __attribute__((ext_vector_type(4)));
typedef float f32x16 __attribute__((ext_vector_type(16)));
typedef unsigned u32x2 __attribute__((ext_vector_type(2)));
typedef unsigned u32x4 __attribute__((ext_vector_type(4)));
typedef unsigned u32x4u __attribute__((ext_vector_type(4), aligned(2)));

constexpr int S_ = 2048, D_ = 1024, F_ = 3584, DF_ = 513;
constexpr size_t WS_XN = 0;
constexpr size_t WS_H = 4u << 20;
constexpr size_t WS_RB = 20u << 20;
constexpr size_t WS_WB = 24u << 20;
constexpr size_t WS_Z = 29u << 20;
constexpr size_t WS_BASE = 34u << 20;

DI unsigned pack2(float a, float b) { f32x2 v = {a, b}; return __builtin_bit_cast(unsigned, __builtin_convertvector(v, bf16x2)); }
DI float bf_lo(unsigned u) { return __uint_as_float(u << 16); }
DI float bf_hi(unsigned u) { return __uint_as_float(u & 0xffff0000u); }
DI float rbf(float a) { return bf_lo(pack2(a, a)); }
DI int crow(int reg, int h) { return (reg & 3) + 8 * (reg >> 2) + 4 * h; }
DI int krow(int s, int h, int j) { return 16 * s + 8 * (j >> 2) + 4 * h + (j & 3); }
DI float cosr(float t) { return __builtin_amdgcn_cosf(t); }
DI float sinr(float t) { return __builtin_amdgcn_sinf(t); }
DI bf16x8 mk8(unsigned a, unsigned b, unsigned c, unsigned d) { u32x4 v = {a, b, c, d}; return __builtin_bit_cast(bf16x8, v); }
DI bf16x8 pack_step(const f32x16& x, int s) { return mk8(pack2(x[8 * s], x[8 * s + 1]), pack2(x[8 * s + 2], x[8 * s + 3]), pack2(x[8 * s + 4], x[8 * s + 5]), pack2(x[8 * s + 6], x[8 * s + 7])); }
DI bf16x4 tr_read(LAS const unsigned char* p) { return __builtin_amdgcn_ds_read_tr16_b64_v4bf16((LAS bf16x4*)p); }
DI bf16x8 cat4(bf16x4 a, bf16x4 b) { return __builtin_shufflevector(a, b, 0, 1, 2, 3, 4, 5, 6, 7); }
#define MFMA32(a, b, c) __builtin_amdgcn_mfma_f32_32x32x16_bf16((a), (b), (c), 0, 0, 0)
#define MFMA16(a, b, c) __builtin_amdgcn_mfma_f32_16x16x32_bf16((a), (b), (c), 0, 0, 0)
#define GLDS16(g, l) __builtin_amdgcn_global_load_lds((const unsigned*)(g), (LAS unsigned*)(l), 16, 0, 0)
#define WAIT_V(n) asm volatile("s_waitcnt vmcnt(" #n ")" ::: "memory")
#define WAIT_L(n) asm volatile("s_waitcnt lgkmcnt(" #n ")" ::: "memory")
#define WAIT_L0 __builtin_amdgcn_s_waitcnt(0xC07F)
#define BAR __builtin_amdgcn_s_barrier()
#define SCHED __builtin_amdgcn_sched_barrier(0)

#define STAMP_DECL
#define STAMP_ARG
#define STAMP_PASS
#define STAMP(s)
#define STAMP_FLUSH(k)
DI f32x16 zero16() { f32x16 z; for (int i = 0; i < 16; ++i) z[i] = 0.f; return z; }
DI float wave_sum(float v) {
#pragma unroll
  for (int o = 32; o >= 1; o >>= 1) v += __shfl_xor(v, o);
  return v;
}

constexpr int K0_XT = 0;
constexpr int K0_RT = 16384;
constexpr int K0_WT = K0_RT + 8 * 2304;
constexpr int K0_LDS = K0_WT + 16 * 1056;
__global__ __launch_bounds__(512) void k0_ln_rowfft(const u16* __restrict__ x, const u16* __restrict__ w_r, const u16* __restrict__ w_i, unsigned char* __restrict__ ws) {
  __shared__ __attribute__((aligned(16))) unsigned char smem_[K0_LDS];
  LAS unsigned char* smem = (LAS unsigned char*)smem_;
  STAMP_DECL; STAMP(0);
  const int tid = threadIdx.x, lane = tid & 63, j = __builtin_amdgcn_readfirstlane(tid >> 6), w = blockIdx.x;
  const int q = w >> 6, s = (w & 63) + 64 * krow(q >> 1, q & 1, j);
  const int r = lane & 31, h = lane >> 5;
  const u32x4 x0 = *(const u32x4*)(x + (size_t)s * D_ + lane * 8), x1 = *(const u32x4*)(x + (size_t)s * D_ + 512 + lane * 8);
  const u32x4 wr = *(const u32x4u*)(w_r + (size_t)s * DF_ + lane * 8), wi = *(const u32x4u*)(w_i + (size_t)s * DF_ + lane * 8);
  u16 wn = 0;
  if (lane < 2) wn = (lane ? w_i : w_r)[(size_t)s * DF_ + 512];
  bf16x8 ec[2], ens[2], a1[2], a2[2];
  float twc[16], tws[16];
#pragma unroll
  for (int ks = 0; ks < 2; ++ks) {
    float c[8], n[8], p[8], m[8];
#pragma unroll
    for (int e = 0; e < 8; ++e) {
      const int a = krow(ks, h, e);
      const float t = (float)((a * r) & 31) * (1.f / 32.f);
      c[e] = cosr(t); n[e] = -sinr(t);
      const float t2 = (float)((a * (r >> 1)) & 31) * (1.f / 32.f);
      const float c2 = cosr(t2), s2 = sinr(t2);
      p[e] = (r & 1) ? -s2 : c2;
      m[e] = (r & 1) ? c2 : s2;
    }
    ec[ks] = mk8(pack2(c[0], c[1]), pack2(c[2], c[3]), pack2(c[4], c[5]), pack2(c[6], c[7]));
    ens[ks] = mk8(pack2(n[0], n[1]), pack2(n[2], n[3]), pack2(n[4], n[5]), pack2(n[6], n[7]));
    a1[ks] = mk8(pack2(p[0], p[1]), pack2(p[2], p[3]), pack2(p[4], p[5]), pack2(p[6], p[7]));
    a2[ks] = mk8(pack2(m[0], m[1]), pack2(m[2], m[3]), pack2(m[4], m[5]), pack2(m[6], m[7]));
  }
#pragma unroll
  for (int i = 0; i < 16; ++i) { const float t = (float)(crow(i, h) * r) * (1.f / 1024.f); twc[i] = cosr(t); tws[i] = sinr(t); }
  STAMP(1);
  float v[16];
#pragma unroll
  for (int i = 0; i < 4; ++i) { v[2 * i] = bf_lo(x0[i]); v[2 * i + 1] = bf_hi(x0[i]); v[8 + 2 * i] = bf_lo(x1[i]); v[9 + 2 * i] = bf_hi(x1[i]); }
  float sum = 0.f;
#pragma unroll
  for (int i = 0; i < 16; ++i) sum += v[i];
  const float mean = wave_sum(sum) * (1.f / 1024.f);
  float sq = 0.f;
#pragma unroll
  for (int i = 0; i < 16; ++i) { const float d = v[i] - mean; sq += d * d; }
  const float var = wave_sum(sq) * (1.f / 1024.f);
  const float mb = rbf(mean), den = rbf(sqrtf(rbf(rbf(var) + 1e-5f)));
  const float rden = 1.0f / den;
  u32x4 n0, n1; float alt = 0.f;
#pragma unroll
  for (int i = 0; i < 4; ++i) {
    const unsigned d0 = pack2(v[2 * i] - mb, v[2 * i + 1] - mb), d1 = pack2(v[8 + 2 * i] - mb, v[9 + 2 * i] - mb);
    n0[i] = pack2(bf_lo(d0) * rden, bf_hi(d0) * rden); n1[i] = pack2(bf_lo(d1) * rden, bf_hi(d1) * rden);
    alt += (bf_lo(n0[i]) - bf_hi(n0[i])) + (bf_lo(n1[i]) - bf_hi(n1[i]));
  }
  STAMP(2);
  u16* xn = (u16*)(ws + WS_XN);
  *(u32x4*)(xn + (size_t)s * D_ + lane * 8) = n0; *(u32x4*)(xn + (size_t)s * D_ + 512 + lane * 8) = n1;
  LAS unsigned char* xt = smem + K0_XT + j * 2048;
  *(LAS u32x4*)(xt + lane * 16) = n0; *(LAS u32x4*)(xt + 1024 + lane * 16) = n1;
  *(LAS u32x4*)(smem + K0_WT + j * 1056 + lane * 16) = wr; *(LAS u32x4*)(smem + K0_WT + (8 + j) * 1056 + lane * 16) = wi;
  const int pos32 = (((w >> 5) * 8 + (w & 7)) * 4 + ((w >> 3) & 3)) * 32;
  if (lane < 2) *(u16*)(ws + WS_WB + (size_t)512 * 8192 + pos32 + lane * 16 + j * 2) = wn;
  alt = wave_sum(alt);
  const int g2 = (lane >> 4) & 1, qq = (lane & 15) >> 2, pp = lane & 3;
  f32x16 tr = zero16(), ti = zero16();
#pragma unroll
  for (int ks = 0; ks < 2; ++ks) {
    const bf16x4 lo = tr_read(xt + (16 * ks + 4 * h + qq) * 64 + g2 * 32 + pp * 8), hi = tr_read(xt + (16 * ks + 8 + 4 * h + qq) * 64 + g2 * 32 + pp * 8);
    const bf16x8 xa = cat4(lo, hi);
    tr = MFMA32(xa, ec[ks], tr); ti = MFMA32(xa, ens[ks], ti);
  }
  f32x16 ur, ui;
#pragma unroll
  for (int i = 0; i < 16; ++i) { ur[i] = tr[i] * twc[i] + ti[i] * tws[i]; ui[i] = ti[i] * twc[i] - tr[i] * tws[i]; }
  f32x16 o = zero16();
#pragma unroll
  for (int ks = 0; ks < 2; ++ks) { o = MFMA32(a1[ks], pack_step(ur, ks), o); o = MFMA32(a2[ks], pack_step(ui, ks), o); }
  if (lane == 0) o[1] = alt;
#pragma unroll
  for (int g = 0; g < 4; ++g) { u32x2 pv = {pack2(o[4 * g], o[4 * g + 1]), pack2(o[4 * g + 2], o[4 * g + 3])}; *(LAS u32x2*)(smem + K0_RT + j * 2304 + r * 72 + (8 * g + 4 * h) * 2) = pv; }
  STAMP(3);
  __syncthreads();
  STAMP(4);
  const int i16 = lane & 15, g4 = lane >> 4;
#pragma unroll
  for (int it = 0; it < 2; ++it) {
    const int base = j * 128 + it * 64 + g4 * 16;
    const int src = base + pp * 4;
    LAS const unsigned char* p = smem + K0_RT + qq * 2304 + (src >> 5) * 72 + (src & 31) * 2;
    const bf16x8 c = cat4(tr_read(p), tr_read(p + 4 * 2304));
    const int ch = base + i16;
    *(bf16x8*)(ws + WS_RB + (size_t)(ch >> 1) * 8192 + pos32 + (ch & 1) * 16) = c;
  }
#pragma unroll
  for (int it = 0; it < 2; ++it) {
    const int ka0 = j * 4, kbh = it * 8;
    const int us = g4 * 4 + pp;
    LAS const unsigned char* p = smem + K0_WT + ((us & 1) * 8 + qq) * 1056 + (ka0 + 32 * (kbh + (us >> 1))) * 2;
    const bf16x8 c = cat4(tr_read(p), tr_read(p + 4 * 1056));
    const int ur_ = g4 * 4 + (i16 >> 2), ka = ka0 + (i16 & 3);
    *(bf16x8*)(ws + WS_WB + (size_t)(ka * 16 + kbh + (ur_ >> 1)) * 8192 + pos32 + (ur_ & 1) * 16) = c;
  }
  STAMP(5);
  STAMP_FLUSH(0);
}

struct CF { bf16x8 c[2], s[2]; };
DI void mkcf(CF& f, int mode, int r, int h) {
#pragma unroll
  for (int ks = 0; ks < 2; ++ks) {
    float c[8], s[8];
#pragma unroll
    for (int e = 0; e < 8; ++e) {
      const int a = krow(ks, h, e);
      const int num = mode == 0 ? 2 * a * r : (mode == 1 ? (2 * a + 1) * r : (2 * r + 1) * a);
      const float t = (float)(num & 63) * (1.f / 64.f);
      c[e] = cosr(t); s[e] = sinr(t);
    }
    f.c[ks] = mk8(pack2(c[0], c[1]), pack2(c[2], c[3]), pack2(c[4], c[5]), pack2(c[6], c[7]));
    f.s[ks] = mk8(pack2(s[0], s[1]), pack2(s[2], s[3]), pack2(s[4], s[5]), pack2(s[6], s[7]));
  }
}
DI bf16x8 neg8(bf16x8 v) { u32x4 u = __builtin_bit_cast(u32x4, v); u ^= 0x80008000u; return __builtin_bit_cast(bf16x8, u); }
template <bool DA, bool INV>
DI void cmm(f32x16& re, f32x16& im, const bf16x8 (&xr)[2], const bf16x8 (&xi)[2], const CF& f) {
  re = zero16(); im = zero16();
#pragma unroll
  for (int ks = 0; ks < 2; ++ks) {
    const bf16x8 p = INV ? neg8(xi[ks]) : xi[ks], m = INV ? xr[ks] : neg8(xr[ks]);
    if (DA) { re = MFMA32(xr[ks], f.c[ks], re); im = MFMA32(xi[ks], f.c[ks], im); re = MFMA32(p, f.s[ks], re); im = MFMA32(m, f.s[ks], im); }
    else    { re = MFMA32(f.c[ks], xr[ks], re); im = MFMA32(f.c[ks], xi[ks], im); re = MFMA32(f.s[ks], p, re); im = MFMA32(f.s[ks], m, im); }
  }
}
#define PACK4() do { _Pragma("unroll") for (int ks = 0; ks < 2; ++ks) { pr[0][ks] = pack_step(ar, ks); pi[0][ks] = pack_step(ai, ks); pr[1][ks] = pack_step(br, ks); pi[1][ks] = pack_step(bi, ks); } } while (0)

DI void col_pass(unsigned char* __restrict__ ws, int col0 STAMP_ARG) {
  const int lane = threadIdx.x & 63, r = lane & 31, h = lane >> 5;
  CF e0, e1, e1t; float twc[16], tws[16];
  mkcf(e0, 0, r, h); mkcf(e1, 1, r, h); mkcf(e1t, 2, r, h);
#pragma unroll
  for (int i = 0; i < 16; ++i) { const float t = (float)(crow(i, h) * r) * (1.f / 2048.f); twc[i] = cosr(t) * (1.f / 1024.f); tws[i] = sinr(t) * (1.f / 1024.f); }
  const int lo = (h * 64 + (r & 7) * 4 + (r >> 3)) * 32;
  const int nit = col0 == 0 ? 3 : 2;
  STAMP(1);
#pragma unroll 1
  for (int it = 0; it < nit; ++it) {
    const int col = it == 2 ? 0 : col0 + it, wcol = it == 2 ? 512 : col;
    const bool real = col == 0;
    const unsigned char* rp = ws + WS_RB + (size_t)col * 8192 + lo + (it == 2 ? 16 : 0);
    const unsigned char* wp = ws + WS_WB + (size_t)wcol * 8192 + lo;
    bf16x8 pr[2][2], pi[2][2]; u32x4 wr[2][2], wi[2][2];
#pragma unroll
    for (int b = 0; b < 2; ++b)
#pragma unroll
      for (int ks = 0; ks < 2; ++ks) {
        pr[b][ks] = *(const bf16x8*)(rp + ks * 4096 + b * 1024);
        pi[b][ks] = real ? mk8(0, 0, 0, 0) : *(const bf16x8*)(rp + ks * 4096 + b * 1024 + 16);
      }
#pragma unroll
    for (int b = 0; b < 2; ++b)
#pragma unroll
      for (int ks = 0; ks < 2; ++ks) { wr[b][ks] = *(const u32x4*)(wp + ks * 4096 + b * 1024); wi[b][ks] = *(const u32x4*)(wp + ks * 4096 + b * 1024 + 16); }
    f32x16 ar, ai, br, bi;
    if (it == 0) { WAIT_V(0); STAMP(2); } else { WAIT_V(0); STAMP(4); }
    cmm<true, false>(ar, ai, pr[0], pi[0], e0);
    cmm<true, false>(br, bi, pr[1], pi[1], e1);
#pragma unroll
    for (int i = 0; i < 16; ++i) {
      const float sr = ar[i] + br[i], si = ai[i] + bi[i], dr = ar[i] - br[i], di = ai[i] - bi[i];
      ar[i] = sr * twc[i] + si * tws[i]; ai[i] = si * twc[i] - sr * tws[i];
      br[i] = dr * twc[i] + di * tws[i]; bi[i] = di * twc[i] - dr * tws[i];
    }
    PACK4();
    cmm<false, false>(ar, ai, pr[0], pi[0], e0);
    cmm<false, false>(br, bi, pr[1], pi[1], e1t);
#pragma unroll
    for (int i = 0; i < 16; ++i) {
      const unsigned u0r = wr[0][i >> 3][(i >> 1) & 3], u0i = wi[0][i >> 3][(i >> 1) & 3], u1r = wr[1][i >> 3][(i >> 1) & 3], u1i = wi[1][i >> 3][(i >> 1) & 3];
      const float w0r = (i & 1) ? bf_hi(u0r) : bf_lo(u0r), w0i = (i & 1) ? bf_hi(u0i) : bf_lo(u0i), w1r = (i & 1) ? bf_hi(u1r) : bf_lo(u1r), w1i = (i & 1) ? bf_hi(u1i) : bf_lo(u1i);
      const float t0 = ar[i], t1 = br[i];
      ar[i] = t0 * w0r - ai[i] * w0i; ai[i] = t0 * w0i + ai[i] * w0r;
      br[i] = t1 * w1r - bi[i] * w1i; bi[i] = t1 * w1i + bi[i] * w1r;
    }
    PACK4();
    cmm<true, true>(ar, ai, pr[0], pi[0], e0);
    cmm<true, true>(br, bi, pr[1], pi[1], e1);
#pragma unroll
    for (int i = 0; i < 16; ++i) {
      const float sr = ar[i] + br[i], si = ai[i] + bi[i], dr = ar[i] - br[i], di = ai[i] - bi[i];
      ar[i] = sr * twc[i] - si * tws[i]; ai[i] = si * twc[i] + sr * tws[i];
      br[i] = dr * twc[i] - di * tws[i]; bi[i] = di * twc[i] + dr * tws[i];
    }
    PACK4();
    cmm<false, true>(ar, ai, pr[0], pi[0], e0);
    cmm<false, true>(br, bi, pr[1], pi[1], e1t);
    const float sc = real ? 0.5f : 1.f;
    unsigned char* zp = ws + WS_Z + (size_t)wcol * 8192 + lane * 16;
#pragma unroll
    for (int g = 0; g < 4; ++g) {
      u32x4 o0, o1;
#pragma unroll
      for (int n = 0; n < 4; ++n) { o0[n] = pack2(ar[4 * g + n] * sc, ai[4 * g + n]); o1[n] = pack2(br[4 * g + n] * sc, bi[4 * g + n]); }
      *(u32x4*)(zp + g * 2048) = o0; *(u32x4*)(zp + g * 2048 + 1024) = o1;
    }
    if (it == 0) { STAMP(3); } else { STAMP(5); }
  }
}
__global__ __launch_bounds__(512, 2) void k_col(unsigned char* __restrict__ ws) {
  const int wv = __builtin_amdgcn_readfirstlane(threadIdx.x >> 6);
  STAMP_DECL; STAMP(0);
  col_pass(ws, blockIdx.x * 16 + wv * 2 STAMP_PASS);
  STAMP_FLUSH(1);
}

DI void row_inv(LAS unsigned char* smem, const u16* __restrict__ x, unsigned char* __restrict__ ws, int c STAMP_ARG) {
  const int lane = threadIdx.x & 63, r = lane & 31, h = lane >> 5, wv = __builtin_amdgcn_readfirstlane(threadIdx.x >> 6);
#pragma unroll
  for (int i = 0; i < 16; ++i) {
    const int col = (wv * 16 + i) * 4 + (lane >> 4);
    GLDS16(ws + WS_Z + (size_t)col * 8192 + c * 256 + (((lane & 15) ^ ((col >> 4) & 15)) * 16), smem + (wv * 16 + i) * 1024);
  }
  const int g = c >> 3, bz = (c >> 2) & 1, hz = (c >> 1) & 1;
  u32x4 xx[2][4][2]; u32x4 nyq[2];
#pragma unroll
  for (int o = 0; o < 2; ++o) {
    const int ro = wv + 8 * o;
    nyq[o] = *(const u32x4*)(ws + WS_Z + (size_t)512 * 8192 + c * 256 + ro * 16);
#pragma unroll
    for (int n = 0; n < 4; ++n) {
      const int tok = 64 * (8 * g + 4 * hz + n) + 32 * bz + (c & 1) * 16 + ro;
      const u16* px = x + (size_t)tok * D_ + r * 32 + h * 16;
      xx[o][n][0] = *(const u32x4*)px; xx[o][n][1] = *(const u32x4*)(px + 8);
    }
  }
  const int b = 16 * ((r >> 2) & 1) + (r & 3) + 4 * (r >> 3);
  bf16x8 b1[2], b2[2], ec[2], ens[2]; float twc[16], tws[16];
#pragma unroll
  for (int ks = 0; ks < 2; ++ks) {
    float p[8], m[8], cc[8], nn[8];
#pragma unroll
    for (int e = 0; e < 8; ++e) {
      const int kb = 8 * h + 4 * ks + (e >> 1);
      const float t = (float)((kb * b) & 31) * (1.f / 32.f);
      const float c1 = cosr(t), s1 = sinr(t);
      p[e] = (e & 1) ? -s1 : c1; m[e] = (e & 1) ? c1 : s1;
      const float t2 = (float)((krow(ks, h, e) * r) & 31) * (1.f / 32.f);
      cc[e] = cosr(t2); nn[e] = -sinr(t2);
    }
    b1[ks] = mk8(pack2(p[0], p[1]), pack2(p[2], p[3]), pack2(p[4], p[5]), pack2(p[6], p[7]));
    b2[ks] = mk8(pack2(m[0], m[1]), pack2(m[2], m[3]), pack2(m[4], m[5]), pack2(m[6], m[7]));
    ec[ks] = mk8(pack2(cc[0], cc[1]), pack2(cc[2], cc[3]), pack2(cc[4], cc[5]), pack2(cc[6], cc[7]));
    ens[ks] = mk8(pack2(nn[0], nn[1]), pack2(nn[2], nn[3]), pack2(nn[4], nn[5]), pack2(nn[6], nn[7]));
  }
#pragma unroll
  for (int i = 0; i < 16; ++i) { const float t = (float)(crow(i, h) * b) * (1.f / 1024.f); twc[i] = cosr(t); tws[i] = sinr(t); }
  STAMP(1);
  WAIT_V(0); BAR;
  STAMP(2);
#pragma unroll
  for (int o = 0; o < 2; ++o) {
    const int ro = wv + 8 * o;
    u32x4 z[2][4];
#pragma unroll
    for (int ks = 0; ks < 2; ++ks)
#pragma unroll
      for (int u = 0; u < 4; ++u) z[ks][u] = *(LAS const u32x4*)(smem + (r * 16 + 8 * h + 4 * ks + u) * 256 + ((ro ^ (r & 15)) * 16));
#pragma unroll
    for (int n = 0; n < 4; ++n) {
      f32x16 ur = zero16(), ui = zero16();
#pragma unroll
      for (int ks = 0; ks < 2; ++ks) { const bf16x8 za = mk8(z[ks][0][n], z[ks][1][n], z[ks][2][n], z[ks][3][n]); ur = MFMA32(za, b1[ks], ur); ui = MFMA32(za, b2[ks], ui); }
      const float ny = bf_lo(nyq[o][n]);
      f32x16 vr, vi;
#pragma unroll
      for (int i = 0; i < 16; ++i) { vr[i] = ur[i] * twc[i] - ui[i] * tws[i]; vi[i] = ui[i] * twc[i] + ur[i] * tws[i]; }
      f32x16 y;
#pragma unroll
      for (int i = 0; i < 16; ++i) y[i] = (i & 1) ? -ny : ny;
#pragma unroll
      for (int ks = 0; ks < 2; ++ks) { y = MFMA32(pack_step(vr, ks), ec[ks], y); y = MFMA32(pack_step(vi, ks), ens[ks], y); }
      u32x4 ov[2];
#pragma unroll
      for (int i = 0; i < 8; ++i) {
        const unsigned yb = pack2(y[2 * i], y[2 * i + 1]), xb = xx[o][n][i >> 2][i & 3];
        ov[i >> 2][i & 3] = pack2(bf_lo(xb) + bf_lo(yb), bf_hi(xb) + bf_hi(yb));
      }
      const int tok = 64 * (8 * g + 4 * hz + n) + 32 * bz + (c & 1) * 16 + ro;
      u16* po = (u16*)(ws + WS_BASE) + (size_t)tok * D_ + r * 32 + h * 16;
      *(u32x4*)po = ov[0]; *(u32x4*)(po + 8) = ov[1];
    }
  }
}
__global__ __launch_bounds__(512, 2) void k_rowinv(const u16* __restrict__ x, unsigned char* __restrict__ ws) {
  extern __shared__ __attribute__((aligned(16))) unsigned char smem_[];
  STAMP_DECL; STAMP(0);
  row_inv((LAS unsigned char*)smem_, x, ws, blockIdx.x STAMP_PASS);
  STAMP(3);
  STAMP_FLUSH(2);
}

constexpr int HTB = 16384;
DI float geglu(float g, float u) {
  const float k1 = -2.f * 0.7978845608028654f * 1.4426950408889634f, k2 = k1 * 0.044715f;
  const float e = __builtin_amdgcn_exp2f(g * (k1 + k2 * g * g));
  return g * u * __builtin_amdgcn_rcpf(1.f + e);
}
DI void gemm1_tile(LAS unsigned char* lds, const u16* __restrict__ xn, const u16* __restrict__ wg, const u16* __restrict__ wu, unsigned char* __restrict__ hbuf, int pm, int pn STAMP_ARG) {
  const int tid = threadIdx.x, wid = __builtin_amdgcn_readfirstlane(tid >> 6), lane = tid & 63, wr = wid >> 2, wc = wid & 3, fr = lane & 15, fq = lane >> 4;
  unsigned voffA[2], voffB[2];
#pragma unroll
  for (int i = 0; i < 2; ++i) {
    const int b = tid * 16 + i * 8192;
    const int ra = b >> 7, ca = ((b >> 4) & 7) ^ ((ra >> 1) & 7);
    voffA[i] = (unsigned)(ra * D_ + ca * 8) * 2u;
    const int kb = b >> 8, p = (b >> 4) & 15, u = (p >> 1) ^ ((kb & 3) | (((kb >> 3) & 1) << 2));
    voffB[i] = (unsigned)(kb * F_ + u * 16 + (p & 1) * 8) * 2u;
  }
  const unsigned ldsw = (unsigned)wid * 1024u;
  const int aoff0 = (wr * 64 + fr) * 128 + ((fq ^ (fr >> 1)) * 16), aoff1 = aoff0 ^ 64;
  const int q4 = (lane & 15) >> 2, pp = lane & 3;
  const int boff0 = (8 * fq + q4) * 256 + (((wc * 2) ^ (q4 | ((fq & 1) << 2))) * 32) + pp * 8, boff1 = boff0 ^ 32;
  const char* cA = (const char*)xn + (size_t)pm * 256 * D_ * 2;
  const char* cG = (const char*)wg + (size_t)pn * 128 * 2;
  const char* cU = (const char*)wu + (size_t)pn * 128 * 2;
  constexpr size_t hA = (size_t)128 * D_ * 2, kA = 128, kB = (size_t)64 * F_ * 2;
#define SA(b, h) (((b) * 2 + (h)) * HTB)
#define SB(b, h) ((4 + (b) * 2 + (h)) * HTB)
#define STAGE(bufoff, gbase, voff) do { _Pragma("unroll") for (int _i = 0; _i < 2; ++_i) GLDS16((const char*)(gbase) + (voff)[_i], lds + (bufoff) + ldsw + _i * 8192); } while (0)
#define LDA(dst, b, h) do { _Pragma("unroll") for (int m = 0; m < 4; ++m) { dst[m][0] = *(const LAS bf16x8*)(lds + SA(b, h) + aoff0 + m * 2048); dst[m][1] = *(const LAS bf16x8*)(lds + SA(b, h) + aoff1 + m * 2048); } } while (0)
#define LDB(dst, b, h) do { _Pragma("unroll") for (int k = 0; k < 2; ++k) { \
    dst[0][k] = cat4(tr_read(lds + SB(b, h) + boff0 + k * 8192), tr_read(lds + SB(b, h) + boff0 + k * 8192 + 1024)); \
    dst[1][k] = cat4(tr_read(lds + SB(b, h) + boff1 + k * 8192), tr_read(lds + SB(b, h) + boff1 + k * 8192 + 1024)); } } while (0)
#define MMA(ai, bj, At, Bt) do { __builtin_amdgcn_s_setprio(1); _Pragma("unroll") for (int m = 0; m < 4; ++m) _Pragma("unroll") for (int n = 0; n < 2; ++n) _Pragma("unroll") for (int k = 0; k < 2; ++k) \
    acc[ai][bj][m][n] = MFMA16(Bt[n][k], At[m][k], acc[ai][bj][m][n]); __builtin_amdgcn_s_setprio(0); } while (0)
  f32x4 acc[2][2][4][2];
#pragma unroll
  for (int a = 0; a < 2; ++a)
#pragma unroll
    for (int b = 0; b < 2; ++b)
#pragma unroll
      for (int m = 0; m < 4; ++m)
#pragma unroll
        for (int n = 0; n < 2; ++n) acc[a][b][m][n] = (f32x4){0.f, 0.f, 0.f, 0.f};
  bf16x8 At[4][2], B0[2][2], B1[2][2];
  constexpr int nt = D_ / 64;
  STAGE(SB(0, 0), cG, voffB); STAGE(SA(0, 0), cA, voffA); STAGE(SB(0, 1), cU, voffB); STAGE(SA(0, 1), cA + hA, voffA);
  if (wr == 1) BAR;
  WAIT_V(4); BAR;
  STAGE(SB(1, 0), cG + kB, voffB); STAGE(SA(1, 0), cA + kA, voffA); STAGE(SB(1, 1), cU + kB, voffB);
  WAIT_V(6); BAR;
  STAMP(1);
  for (int t = 0; t < nt - 2; t += 2) {
    const char* a1 = cA + (size_t)(t + 1) * kA; const char* a2 = a1 + kA; const char* a3 = a2 + kA;
    const char* g2 = cG + (size_t)(t + 2) * kB; const char* g3 = g2 + kB; const char* u2 = cU + (size_t)(t + 2) * kB; const char* u3 = u2 + kB;
    LDB(B0, 0, 0); SCHED; LDA(At, 0, 0); STAGE(SA(1, 1), a1 + hA, voffA);
    WAIT_L(8); BAR; WAIT_L(0); MMA(0, 0, At, B0); BAR; SCHED;
    LDB(B1, 0, 1); STAGE(SB(0, 0), g2, voffB);
    BAR; WAIT_L(0); MMA(0, 1, At, B1); BAR;
    LDA(At, 0, 1); STAGE(SA(0, 0), a2, voffA);
    BAR; WAIT_L(0); MMA(1, 0, At, B0); BAR; SCHED;
    STAGE(SB(0, 1), u2, voffB);
    WAIT_V(6); BAR; MMA(1, 1, At, B1); BAR;
    LDB(B0, 1, 0); SCHED; LDA(At, 1, 0); STAGE(SA(0, 1), a2 + hA, voffA);
    WAIT_L(8); BAR; WAIT_L(0); MMA(0, 0, At, B0); BAR; SCHED;
    LDB(B1, 1, 1); STAGE(SB(1, 0), g3, voffB);
    BAR; WAIT_L(0); MMA(0, 1, At, B1); BAR;
    LDA(At, 1, 1); STAGE(SA(1, 0), a3, voffA);
    BAR; WAIT_L(0); MMA(1, 0, At, B0); BAR; SCHED;
    STAGE(SB(1, 1), u3, voffB);
    WAIT_V(6); BAR; MMA(1, 1, At, B1); BAR;
  }
  STAMP(2);
  { LDB(B0, 0, 0); WAIT_V(0); LDA(At, 0, 0); STAGE(SA(1, 1), cA + (size_t)(nt - 1) * kA + hA, voffA);
    BAR; WAIT_L(0); MMA(0, 0, At, B0); BAR;
    LDB(B1, 0, 1); BAR; WAIT_L(0); MMA(0, 1, At, B1); BAR;
    LDA(At, 0, 1); BAR; WAIT_L(0); MMA(1, 0, At, B0); MMA(1, 1, At, B1); BAR; }
  { LDB(B0, 1, 0); LDA(At, 1, 0); BAR; WAIT_L(0); MMA(0, 0, At, B0); BAR;
    LDB(B1, 1, 1); WAIT_V(0); BAR; WAIT_L(0); MMA(0, 1, At, B1); BAR;
    LDA(At, 1, 1); BAR; WAIT_L(0); MMA(1, 0, At, B0); MMA(1, 1, At, B1); BAR; }
  if (wr == 0) BAR;
  STAMP(3);
#pragma unroll
  for (int ai = 0; ai < 2; ++ai)
#pragma unroll
    for (int m = 0; m < 4; ++m) {
      u32x4 o;
#pragma unroll
      for (int n = 0; n < 2; ++n) {
        const f32x4 g = acc[ai][0][m][n], u = acc[ai][1][m][n];
        o[2 * n] = pack2(geglu(g[0], u[0]), geglu(g[1], u[1])); o[2 * n + 1] = pack2(geglu(g[2], u[2]), geglu(g[3], u[3]));
      }
      *(u32x4*)(hbuf + ((size_t)((pm * 2 + ai) * 112 + pn * 4 + wc) * 8 + wr * 4 + m) * 1024 + lane * 16) = o;
    }
#undef SA
#undef SB
#undef STAGE
#undef LDA
#undef LDB
#undef MMA
}
__global__ __launch_bounds__(512, 2) void k1_gemm1(const u16* __restrict__ wg, const u16* __restrict__ wu, const u16* __restrict__ wd, unsigned char* __restrict__ ws) {
  extern __shared__ __attribute__((aligned(16))) unsigned char smem_[];
  const int xcd = blockIdx.x & 7, idx = blockIdx.x >> 3;
  if (idx >= 28) {
    const int sp = (idx - 28) * 8 + xcd, wv = __builtin_amdgcn_readfirstlane(threadIdx.x >> 6);
    const unsigned char* p = (const unsigned char*)wd + (size_t)sp * 229376 + threadIdx.x * 16;
#pragma unroll
    for (int i = 0; i < 28; ++i) GLDS16(p + i * 8192, (LAS unsigned char*)smem_ + (i & 15) * 8192 + wv * 1024);
    WAIT_V(0);
    return;
  }
  const int pm = (xcd & 1) * 4 + (idx & 3), pn = (xcd >> 1) * 7 + (idx >> 2);
  STAMP_DECL; STAMP(0);
  gemm1_tile((LAS unsigned char*)smem_, (const u16*)(ws + WS_XN), wg, wu, ws + WS_H, pm, pn STAMP_PASS);
  STAMP(4);
  STAMP_FLUSH(3);
}

constexpr int G2_SLOT = 24576, G2_NS = 6, G2_NT = F_ / 64;
__global__ __launch_bounds__(512, 2) void k2_gemm2(const u16* __restrict__ wd, const unsigned char* __restrict__ ws, u16* __restrict__ out) {
  extern __shared__ __attribute__((aligned(16))) unsigned char smem_[];
  LAS unsigned char* lds = (LAS unsigned char*)smem_;
  const int tid = threadIdx.x, wid = __builtin_amdgcn_readfirstlane(tid >> 6), lane = tid & 63;
  const int xcd = blockIdx.x & 7, idx = blockIdx.x >> 3;
  const int tm = (xcd >> 1) * 4 + (idx & 3), tn = (xcd & 1) * 8 + (idx >> 2);
  STAMP_DECL; STAMP(0);
  const int tok = tid >> 2, dq = tid & 3;
  const size_t go = (size_t)(tm * 128 + tok) * D_ + tn * 64 + dq * 16;
  u32x4 b0, b1;
  if (wid >= 4) {
    const int lw = wid - 4, lt = tid - 256;
    const unsigned char* hsrc = ws + WS_H + (size_t)tm * 112 * 8192 + lt * 16;
    const int wrow = lt >> 3, wp = lt & 7;
    const unsigned char* wsrc = (const unsigned char*)wd + ((size_t)wrow * D_ + tn * 64 + (((wp >> 1) ^ ((wrow >> 1) & 3)) * 16) + (wp & 1) * 8) * 2;
    const unsigned ldsw = lw * 1024u;
#define STAGE2(t, slot) do { _Pragma("unroll") for (int i_ = 0; i_ < 2; ++i_) GLDS16(wsrc + (size_t)(t) * (64 * D_ * 2) + i_ * (32 * D_ * 2), lds + (slot) * G2_SLOT + 16384 + i_ * 4096 + ldsw); \
      _Pragma("unroll") for (int i_ = 0; i_ < 4; ++i_) GLDS16(hsrc + (size_t)(t) * 16384 + i_ * 4096, lds + (slot) * G2_SLOT + i_ * 4096 + ldsw); } while (0)
    STAGE2(0, 0); STAGE2(1, 1); STAGE2(2, 2); STAGE2(3, 3); STAGE2(4, 4); STAGE2(5, 5);
    WAIT_V(30); BAR;
    WAIT_V(24); BAR;
    for (int t = 0; t < 48; t += 6) {
      STAGE2(t + 6, 0); WAIT_V(24); BAR;
      STAGE2(t + 7, 1); WAIT_V(24); BAR;
      STAGE2(t + 8, 2); WAIT_V(24); BAR;
      STAGE2(t + 9, 3); WAIT_V(24); BAR;
      STAGE2(t + 10, 4); WAIT_V(24); BAR;
      STAGE2(t + 11, 5); WAIT_V(24); BAR;
    }
    STAGE2(54, 0); WAIT_V(24); BAR;
    STAGE2(55, 1); WAIT_V(24); BAR;
    WAIT_V(18); BAR;
    WAIT_V(12); BAR;
    WAIT_V(6); BAR;
    WAIT_V(0); BAR;
    b0 = *(const u32x4*)((const u16*)(ws + WS_BASE) + go); b1 = *(const u32x4*)((const u16*)(ws + WS_BASE) + go + 8);
    BAR;
  } else {
    const int wm = wid >> 1, wn = wid & 1, fq = lane >> 4, fr = lane & 15, q4 = (lane & 15) >> 2, pp = lane & 3;
    const int hoff = wm * 4096 + lane * 16;
    const int woff0 = 16384 + (4 * fq + q4) * 128 + (((wn * 2) ^ ((2 * fq + (q4 >> 1)) & 3)) * 32) + pp * 8, woff1 = woff0 ^ 32;
#define LDH(H, slot, k) do { _Pragma("unroll") for (int m = 0; m < 4; ++m) H[k][m] = *(const LAS bf16x8*)(lds + (slot) * G2_SLOT + (k) * 8192 + hoff + m * 1024); } while (0)
#define LDW(W, slot, k) do { W[k][0] = cat4(tr_read(lds + (slot) * G2_SLOT + (k) * 4096 + woff0), tr_read(lds + (slot) * G2_SLOT + (k) * 4096 + woff0 + 2048)); \
      W[k][1] = cat4(tr_read(lds + (slot) * G2_SLOT + (k) * 4096 + woff1), tr_read(lds + (slot) * G2_SLOT + (k) * 4096 + woff1 + 2048)); } while (0)
#define MM2(H, W, k) do { _Pragma("unroll") for (int m = 0; m < 4; ++m) _Pragma("unroll") for (int n = 0; n < 2; ++n) acc[m][n] = MFMA16(W[k][n], H[k][m], acc[m][n]); } while (0)
    f32x4 acc[4][2];
#pragma unroll
    for (int m = 0; m < 4; ++m) { acc[m][0] = (f32x4){0.f, 0.f, 0.f, 0.f}; acc[m][1] = (f32x4){0.f, 0.f, 0.f, 0.f}; }
    bf16x8 Ha[2][4], Wa[2][2], Hb[2][4], Wb[2][2];
    BAR;
    LDW(Wa, 0, 0); LDH(Ha, 0, 0); LDW(Wa, 0, 1); LDH(Ha, 0, 1);
    WAIT_L0; BAR; SCHED;
    STAMP(1);
#define ITER(nslot, Hc, Wc, Hn, Wn) do { LDW(Wn, nslot, 0); LDH(Hn, nslot, 0); SCHED; MM2(Hc, Wc, 0); SCHED; LDW(Wn, nslot, 1); LDH(Hn, nslot, 1); SCHED; MM2(Hc, Wc, 1); SCHED; WAIT_L0; BAR; SCHED; } while (0)
    for (int t = 0; t < 54; t += 6) {
      ITER(1, Ha, Wa, Hb, Wb);
      ITER(2, Hb, Wb, Ha, Wa);
      ITER(3, Ha, Wa, Hb, Wb);
      ITER(4, Hb, Wb, Ha, Wa);
      ITER(5, Ha, Wa, Hb, Wb);
      ITER(0, Hb, Wb, Ha, Wa);
    }
    STAMP(2);
    b0 = *(const u32x4*)((const u16*)(ws + WS_BASE) + go); b1 = *(const u32x4*)((const u16*)(ws + WS_BASE) + go + 8);
    ITER(1, Ha, Wa, Hb, Wb);
    MM2(Hb, Wb, 0); MM2(Hb, Wb, 1);
    STAMP(3);
#pragma unroll
    for (int m = 0; m < 4; ++m)
#pragma unroll
      for (int n = 0; n < 2; ++n) *(LAS f32x4*)(lds + (wm * 64 + m * 16 + fr) * 272 + (wn * 32 + n * 16 + fq * 4) * 4) = acc[m][n];
  }
  __syncthreads();
  u32x4 o[2];
#pragma unroll
  for (int i = 0; i < 4; ++i) {
    const f32x4 a = *(const LAS f32x4*)(lds + tok * 272 + (dq * 16 + i * 4) * 4);
    const unsigned p0 = pack2(a[0], a[1]), p1 = pack2(a[2], a[3]);
    const unsigned c0 = (i < 2 ? b0 : b1)[(2 * i) & 3], c1 = (i < 2 ? b0 : b1)[(2 * i + 1) & 3];
    o[i >> 1][(2 * i) & 3] = pack2(bf_lo(c0) + bf_lo(p0), bf_hi(c0) + bf_hi(p0));
    o[i >> 1][(2 * i + 1) & 3] = pack2(bf_lo(c1) + bf_lo(p1), bf_hi(c1) + bf_hi(p1));
  }
  *(u32x4*)(out + go) = o[0]; *(u32x4*)(out + go + 8) = o[1];
  STAMP(4);
  STAMP_FLUSH(4);
}

extern "C" void kernel_launch(void* const* d_in, const int* in_sizes, int n_in, void* d_out, int out_size, void* d_ws, size_t ws_size, hipStream_t stream) {
  const u16* x = (const u16*)d_in[0]; const u16* w_r = (const u16*)d_in[1]; const u16* w_i = (const u16*)d_in[2];
  const u16* wg = (const u16*)d_in[4]; const u16* wu = (const u16*)d_in[5]; const u16* wd = (const u16*)d_in[6];
  unsigned char* ws = (unsigned char*)d_ws;
  (void)hipFuncSetAttribute((const void*)k_rowinv, hipFuncAttributeMaxDynamicSharedMemorySize, 131072);
  (void)hipFuncSetAttribute((const void*)k1_gemm1, hipFuncAttributeMaxDynamicSharedMemorySize, 131072);
  (void)hipFuncSetAttribute((const void*)k2_gemm2, hipFuncAttributeMaxDynamicSharedMemorySize, G2_SLOT * G2_NS);
  k0_ln_rowfft<<<256, 512, 0, stream>>>(x, w_r, w_i, ws);
  k_col<<<32, 512, 0, stream>>>(ws);
  k_rowinv<<<32, 512, 131072, stream>>>(x, ws);
  k1_gemm1<<<256, 512, 131072, stream>>>(wg, wu, wd, ws);
  k2_gemm2<<<256, 512, G2_SLOT * G2_NS, stream>>>(wd, ws, (u16*)d_out);
}
```

```cpp
#include <hip/hip_runtime.h>
#include <hip/hip_bf16.h>
#include <stdint.h>

#define LAS __attribute__((address_space(3)))
#define DI __device__ __forceinline__
typedef unsigned short u16;
typedef __bf16 bf16x8 __attribute__((ext_vector_type(8)));
typedef __bf16 bf16x4 __attribute__((ext_vector_type(4)));
typedef __bf16 bf16x2 __attribute__((ext_vector_type(2)));
typedef float f32x2 __attribute__((ext_vector_type(2)));
typedef float f32x4 __attribute__((ext_vector_type(4)));
typedef float f32x16 __attribute__((ext_vector_type(16)));
typedef unsigned u32x2 __attribute__((ext_vector_type(2)));
typedef unsigned u32x4 __attribute__((ext_vector_type(4)));
typedef unsigned u32x4u __attribute__((ext_vector_type(4), aligned(2)));

constexpr int S_ = 2048, D_ = 1024, F_ = 3584, DF_ = 513;
constexpr unsigned WS_CTL = 0;
constexpr unsigned WS_FA = 4096;
constexpr unsigned WS_FZ = 8192;
constexpr unsigned WS_STAT = 16384;
constexpr unsigned WS_H = 4u << 20;
constexpr unsigned WS_RB = 20u << 20;
constexpr unsigned WS_Z = 24u << 20;
constexpr unsigned WS_BASE = 29u << 20;
constexpr unsigned WS_USED = 34u << 20;
typedef __attribute__((address_space(1))) unsigned long long gu64;
#define RLX_AGENT __ATOMIC_RELAXED, __HIP_MEMORY_SCOPE_AGENT
constexpr unsigned SPIN_CAP = 1u << 16;
constexpr int HTB = 16384, K1_SCR = 131072, K1_LDS = 163840;

DI unsigned pack2(float a, float b) { f32x2 v = {a, b}; return __builtin_bit_cast(unsigned, __builtin_convertvector(v, bf16x2)); }
DI float bf_lo(unsigned u) { return __uint_as_float(u << 16); }
DI float bf_hi(unsigned u) { return __uint_as_float(u & 0xffff0000u); }
DI float rbf(float a) { return bf_lo(pack2(a, a)); }
DI int crow(int reg, int h) { return (reg & 3) + 8 * (reg >> 2) + 4 * h; }
DI int krow(int s, int h, int j) { return 16 * s + 8 * (j >> 2) + 4 * h + (j & 3); }
DI float cosr(float t) { return __builtin_amdgcn_cosf(t); }
DI float sinr(float t) { return __builtin_amdgcn_sinf(t); }
DI bf16x8 mk8(unsigned a, unsigned b, unsigned c, unsigned d) { u32x4 v = {a, b, c, d}; return __builtin_bit_cast(bf16x8, v); }
DI bf16x8 pack_step(const f32x16& x, int s) { return mk8(pack2(x[8 * s], x[8 * s + 1]), pack2(x[8 * s + 2], x[8 * s + 3]), pack2(x[8 * s + 4], x[8 * s + 5]), pack2(x[8 * s + 6], x[8 * s + 7])); }
DI bf16x4 tr_read(LAS const unsigned char* p) { return __builtin_amdgcn_ds_read_tr16_b64_v4bf16((LAS bf16x4*)p); }
DI bf16x8 cat4(bf16x4 a, bf16x4 b) { return __builtin_shufflevector(a, b, 0, 1, 2, 3, 4, 5, 6, 7); }
#define MFMA32(a, b, c) __builtin_amdgcn_mfma_f32_32x32x16_bf16((a), (b), (c), 0, 0, 0)
#define MFMA16(a, b, c) __builtin_amdgcn_mfma_f32_16x16x32_bf16((a), (b), (c), 0, 0, 0)
#define GLDS16(g, l) __builtin_amdgcn_global_load_lds((const unsigned*)(g), (LAS unsigned*)(l), 16, 0, 0)
#define DMA16(rsrc, voff, soff, lbase, limm) asm volatile("s_add_i32 m0, %0, %4\n\ts_nop 0\n\tbuffer_load_dwordx4 %1, %2, %3 offen lds" :: "s"(lbase), "v"(voff), "s"(rsrc), "s"(soff), "i"(limm) : "scc", "memory")
#define MKRSRC(p, n) __builtin_amdgcn_make_buffer_rsrc((void*)(p), 0, (n), 0x00020000)
#define FENCE_C asm volatile("" ::: "memory")
#define WAIT_V(n) asm volatile("s_waitcnt vmcnt(" #n ")" ::: "memory")
#define WAIT_L(n) asm volatile("s_waitcnt lgkmcnt(" #n ")" ::: "memory")
#define WAIT_L0 __builtin_amdgcn_s_waitcnt(0xC07F)
#define BAR __builtin_amdgcn_s_barrier()
#define SCHED __builtin_amdgcn_sched_barrier(0)

#define STAMP_DECL
#define STAMP_ARG
#define STAMP_PASS
#define STAMP(s)
#define STAMP_FLUSH(k)
#define PIN(v) asm volatile("" : "+v"(v))
#define PIN16(a) do { _Pragma("unroll") for (int i_ = 0; i_ < 16; ++i_) PIN(a[i_]); } while (0)
DI f32x16 zero16() { f32x16 z; for (int i = 0; i < 16; ++i) z[i] = 0.f; return z; }
template <int CTRL, int RM> DI float dpp_add(float v) { return v + __builtin_bit_cast(float, __builtin_amdgcn_update_dpp(0, __builtin_bit_cast(int, v), CTRL, RM, 0xf, true)); }
DI float wave_sum(float v) {
  v = dpp_add<0x111, 0xf>(v); v = dpp_add<0x112, 0xf>(v); v = dpp_add<0x114, 0xf>(v); v = dpp_add<0x118, 0xf>(v);
  v = dpp_add<0x142, 0xa>(v); v = dpp_add<0x143, 0xc>(v);
  return __builtin_bit_cast(float, __builtin_amdgcn_readlane(__builtin_bit_cast(int, v), 63));
}
DI u32x4 ld_sc1(__amdgpu_buffer_rsrc_t rs, unsigned off) { return __builtin_amdgcn_raw_buffer_load_b128(rs, off, 0, 16); }
DI void st_sc1(__amdgpu_buffer_rsrc_t rs, unsigned off, u32x4 v) { __builtin_amdgcn_raw_buffer_store_b128(v, rs, off, 0, 16); }
template <int N> DI void poll_flags(const unsigned char* ws, unsigned off, unsigned long long ep) {
  const int lane = threadIdx.x & 63;
  gu64* f = (gu64*)(ws + off) + (N ? lane : (lane & 31));
#pragma unroll 1
  for (unsigned spins = 0; spins < SPIN_CAP; ++spins) {
    bool ok = true;
#pragma unroll
    for (int k = 0; k < (N ? N : 1); ++k) ok &= __hip_atomic_load(f + 64 * k, RLX_AGENT) == ep;
    if (__all(ok)) break;
    __builtin_amdgcn_s_sleep(2);
  }
  __builtin_amdgcn_fence(__ATOMIC_ACQUIRE, "wavefront");
}

DI unsigned long long next_epoch(unsigned long long e_in) {
  const unsigned long long ep = (e_in ^ 0x9E3779B97F4A7C15ull) * 0xBF58476D1CE4E5B9ull + 0x94D049BB133111EBull;
  return ((unsigned long long)__builtin_amdgcn_readfirstlane((unsigned)(ep >> 32)) << 32) | __builtin_amdgcn_readfirstlane((unsigned)ep);
}
template <class F>
DI void row_fwd(F&& after_load, LAS unsigned char* scr, u32x4 x0, u32x4 x1, unsigned char* __restrict__ ws, __amdgpu_buffer_rsrc_t rs, unsigned long long e_in, unsigned long long& ep, int w, int s STAMP_ARG) {
  const int tid = threadIdx.x, lane = tid & 63, j = __builtin_amdgcn_readfirstlane(tid >> 6);
  const int r = lane & 31, h = lane >> 5;
  bf16x8 ec[2], ens[2], a1[2], a2[2];
  float twc[16], tws[16];
#pragma unroll
  for (int ks = 0; ks < 2; ++ks) {
    float c[8], n[8], p[8], m[8];
#pragma unroll
    for (int e = 0; e < 8; ++e) {
      const int a = krow(ks, h, e);
      const float t = (float)((a * r) & 31) * (1.f / 32.f);
      c[e] = cosr(t); n[e] = -sinr(t);
      const float t2 = (float)((a * (r >> 1)) & 31) * (1.f / 32.f);
      p[e] = cosr(t2 + (float)(r & 1) * 0.25f);
      m[e] = sinr(t2 + (float)(r & 1) * 0.25f);
    }
    ec[ks] = mk8(pack2(c[0], c[1]), pack2(c[2], c[3]), pack2(c[4], c[5]), pack2(c[6], c[7]));
    ens[ks] = mk8(pack2(n[0], n[1]), pack2(n[2], n[3]), pack2(n[4], n[5]), pack2(n[6], n[7]));
    a1[ks] = mk8(pack2(p[0], p[1]), pack2(p[2], p[3]), pack2(p[4], p[5]), pack2(p[6], p[7]));
    a2[ks] = mk8(pack2(m[0], m[1]), pack2(m[2], m[3]), pack2(m[4], m[5]), pack2(m[6], m[7]));
  }
#pragma unroll
  for (int i = 0; i < 16; ++i) { const float t = (float)(crow(i, h) * r) * (1.f / 1024.f); twc[i] = cosr(t); tws[i] = sinr(t); }
  PIN(ec[0]); PIN(ec[1]); PIN(ens[0]); PIN(ens[1]); PIN(a1[0]); PIN(a1[1]); PIN(a2[0]); PIN(a2[1]); PIN16(twc); PIN16(tws);
  asm volatile("" : "+v"(x0), "+v"(x1), "+v"(e_in) :: "memory");
  SCHED; after_load(); SCHED;
  ep = next_epoch(e_in);
  const unsigned tag = (unsigned)(ep >> 32);
  float v[16];
#pragma unroll
  for (int i = 0; i < 4; ++i) { v[2 * i] = bf_lo(x0[i]); v[2 * i + 1] = bf_hi(x0[i]); v[8 + 2 * i] = bf_lo(x1[i]); v[9 + 2 * i] = bf_hi(x1[i]); }
  float sum = 0.f;
#pragma unroll
  for (int i = 0; i < 16; ++i) sum += v[i];
  const float mean = wave_sum(sum) * (1.f / 1024.f);
  float sq = 0.f;
#pragma unroll
  for (int i = 0; i < 16; ++i) { const float d = v[i] - mean; sq += d * d; }
  const float var = wave_sum(sq) * (1.f / 1024.f);
  const float mb = rbf(mean), den = rbf(sqrtf(rbf(rbf(var) + 1e-5f)));
  const float rden = 1.0f / den;
  u32x4 n0, n1; float alt = 0.f;
#pragma unroll
  for (int i = 0; i < 4; ++i) {
    const unsigned d0 = pack2(v[2 * i] - mb, v[2 * i + 1] - mb), d1 = pack2(v[8 + 2 * i] - mb, v[9 + 2 * i] - mb);
    n0[i] = pack2(bf_lo(d0) * rden, bf_hi(d0) * rden); n1[i] = pack2(bf_lo(d1) * rden, bf_hi(d1) * rden);
    alt += (bf_lo(n0[i]) - bf_hi(n0[i])) + (bf_lo(n1[i]) - bf_hi(n1[i]));
  }
  LAS unsigned char* xt = scr + j * 2304;
  *(LAS u32x4*)(xt + lane * 16) = n0; *(LAS u32x4*)(xt + 1024 + lane * 16) = n1;
  if (lane == 0) __hip_atomic_store((gu64*)(ws + WS_STAT) + s, ((unsigned long long)tag << 32) | pack2(mb, den), RLX_AGENT);
  alt = wave_sum(alt);
  const int g2 = (lane >> 4) & 1, qq = (lane & 15) >> 2, pp = lane & 3;
  f32x16 tr = zero16(), ti = zero16();
#pragma unroll
  for (int ks = 0; ks < 2; ++ks) {
    const bf16x4 lo = tr_read(xt + (16 * ks + 4 * h + qq) * 64 + g2 * 32 + pp * 8), hi = tr_read(xt + (16 * ks + 8 + 4 * h + qq) * 64 + g2 * 32 + pp * 8);
    const bf16x8 xa = cat4(lo, hi);
    tr = MFMA32(xa, ec[ks], tr); ti = MFMA32(xa, ens[ks], ti);
  }
  f32x16 ur, ui;
#pragma unroll
  for (int i = 0; i < 16; ++i) { ur[i] = tr[i] * twc[i] + ti[i] * tws[i]; ui[i] = ti[i] * twc[i] - tr[i] * tws[i]; }
  f32x16 o = zero16();
#pragma unroll
  for (int ks = 0; ks < 2; ++ks) { o = MFMA32(a1[ks], pack_step(ur, ks), o); o = MFMA32(a2[ks], pack_step(ui, ks), o); }
  if (lane == 0) o[1] = alt;
#pragma unroll
  for (int g = 0; g < 4; ++g) { u32x2 pv = {pack2(o[4 * g], o[4 * g + 1]), pack2(o[4 * g + 2], o[4 * g + 3])}; *(LAS u32x2*)(xt + r * 72 + (8 * g + 4 * h) * 2) = pv; }
  WAIT_L0; BAR; SCHED;
  const int i16 = lane & 15, g4 = lane >> 4;
  const int pos32 = (((w >> 5) * 8 + (w & 7)) * 4 + ((w >> 3) & 3)) * 32;
#pragma unroll
  for (int it = 0; it < 2; ++it) {
    const int base = j * 128 + it * 64 + g4 * 16;
    const int src = base + pp * 4;
    LAS const unsigned char* p = scr + qq * 2304 + (src >> 5) * 72 + (src & 31) * 2;
    const bf16x8 c = cat4(tr_read(p), tr_read(p + 4 * 2304));
    const int ch = base + i16;
    st_sc1(rs, WS_RB + ((ch >> 5) + 32 * ((ch >> 1) & 15)) * 8192 + pos32 + (ch & 1) * 16, __builtin_bit_cast(u32x4, c));
  }
}

struct CF { bf16x8 c[2], s[2]; };
DI void mkcf(CF& f, int mode, int r, int h) {
#pragma unroll
  for (int ks = 0; ks < 2; ++ks) {
    float c[8], s[8];
#pragma unroll
    for (int e = 0; e < 8; ++e) {
      const int a = krow(ks, h, e);
      const int num = mode == 0 ? 2 * a * r : (mode == 1 ? (2 * a + 1) * r : (2 * r + 1) * a);
      const float t = (float)(num & 63) * (1.f / 64.f);
      c[e] = cosr(t); s[e] = sinr(t);
    }
    f.c[ks] = mk8(pack2(c[0], c[1]), pack2(c[2], c[3]), pack2(c[4], c[5]), pack2(c[6], c[7]));
    f.s[ks] = mk8(pack2(s[0], s[1]), pack2(s[2], s[3]), pack2(s[4], s[5]), pack2(s[6], s[7]));
  }
}
DI bf16x8 neg8(bf16x8 v) { u32x4 u = __builtin_bit_cast(u32x4, v); u ^= 0x80008000u; return __builtin_bit_cast(bf16x8, u); }
template <bool DA, bool INV>
DI void cmm(f32x16& re, f32x16& im, const bf16x8 (&xr)[2], const bf16x8 (&xi)[2], const CF& f) {
  re = zero16(); im = zero16();
#pragma unroll
  for (int ks = 0; ks < 2; ++ks) {
    const bf16x8 p = INV ? neg8(xi[ks]) : xi[ks], m = INV ? xr[ks] : neg8(xr[ks]);
    if (DA) { re = MFMA32(xr[ks], f.c[ks], re); im = MFMA32(xi[ks], f.c[ks], im); re = MFMA32(p, f.s[ks], re); im = MFMA32(m, f.s[ks], im); }
    else    { re = MFMA32(f.c[ks], xr[ks], re); im = MFMA32(f.c[ks], xi[ks], im); re = MFMA32(f.s[ks], p, re); im = MFMA32(f.s[ks], m, im); }
  }
}
#define PACK4() do { _Pragma("unroll") for (int ks = 0; ks < 2; ++ks) { pr[0][ks] = pack_step(ar, ks); pi[0][ks] = pack_step(ai, ks); pr[1][ks] = pack_step(br, ks); pi[1][ks] = pack_step(bi, ks); } } while (0)

constexpr int WI_STR = 8208, WI_NY = 16 * WI_STR;
DI void fft_group(LAS unsigned char* smem, const u16* __restrict__ x, const u16* __restrict__ w_r, const u16* __restrict__ w_i, const u16* __restrict__ wd,
                  unsigned char* __restrict__ ws, __amdgpu_buffer_rsrc_t rs STAMP_ARG) {
  const int tid = threadIdx.x, lane = tid & 63, r = lane & 31, h = lane >> 5, wv = __builtin_amdgcn_readfirstlane(tid >> 6);
  const int w = blockIdx.x, c = (w & 7) * 4 + ((w >> 3) - 28);
  unsigned long long ep;
  {
    const unsigned long long e_in = __hip_atomic_load((gu64*)(ws + WS_CTL), RLX_AGENT);
    const int srow = (w & 63) + 64 * krow(w >> 7, (w >> 6) & 1, wv);
    const u32x4 x0 = *(const u32x4*)(x + (size_t)srow * D_ + lane * 8), x1 = *(const u32x4*)(x + (size_t)srow * D_ + 512 + lane * 8);
    row_fwd([] {}, smem + K1_SCR, x0, x1, ws, rs, e_in, ep, w, srow STAMP_PASS);
  }
  FENCE_C;
  STAMP(1);
  {
    const int sr = lane >> 1, half = lane & 1;
#pragma unroll
    for (int part = 0; part < 2; ++part) {
      u32x4 L[2][4];
      const u16* src = (part ? w_i : w_r) + (size_t)(512 * (wv & 3) + 32 * (wv >> 2) + sr) * DF_ + 16 * c + half * 8;
#pragma unroll
      for (int hh = 0; hh < 2; ++hh)
#pragma unroll
        for (int n = 0; n < 4; ++n) L[hh][n] = *(const u32x4u*)(src + (size_t)(64 * (n + 4 * hh)) * DF_);
      if (part == 0) { WAIT_V(8); BAR; if (tid == 0) __hip_atomic_store((gu64*)(ws + WS_FA) + blockIdx.x, ep, RLX_AGENT); }
#pragma unroll
      for (int hh = 0; hh < 2; ++hh)
#pragma unroll
        for (int q = 0; q < 4; ++q) { u32x4 t = {L[hh][0][q], L[hh][1][q], L[hh][2][q], L[hh][3][q]}; *(LAS u32x4*)(smem + (part * 8 + half * 4 + q) * WI_STR + (wv * 64 + hh * 32 + sr) * 16) = t; }
    }
    if (c == 0) {
#pragma unroll
      for (int part = 0; part < 2; ++part) {
        const u16* src = (part ? w_i : w_r) + (size_t)(512 * (wv & 3) + 32 * (wv >> 2) + 256 * h + r) * DF_ + 512;
        u32x4 t;
#pragma unroll
        for (int n = 0; n < 4; ++n) t[n] = src[(size_t)64 * n * DF_];
        *(LAS u32x4*)(smem + WI_NY + part * 8192 + tid * 16) = t;
      }
    }
  }
  STAMP(2);
  {
    int lane_o = lane; PIN(lane_o);
    const int r = lane_o & 31, h = lane_o >> 5;
    CF e0, e1, e1t; float twc[16], tws[16];
    mkcf(e0, 0, r, h); mkcf(e1, 1, r, h); mkcf(e1t, 2, r, h);
#pragma unroll
    for (int i = 0; i < 16; ++i) { const float t = (float)(crow(i, h) * r) * (1.f / 2048.f); twc[i] = cosr(t) * (1.f / 1024.f); tws[i] = sinr(t) * (1.f / 1024.f); }
#pragma unroll
    for (int ks = 0; ks < 2; ++ks) { PIN(e0.c[ks]); PIN(e0.s[ks]); PIN(e1.c[ks]); PIN(e1.s[ks]); PIN(e1t.c[ks]); PIN(e1t.s[ks]); }
    PIN16(twc); PIN16(tws);
    if (wv == 0) {
      poll_flags<4>(ws, WS_FA, ep);
      if (c == 0 && lane == 0) __hip_atomic_store((gu64*)(ws + WS_CTL), ep, RLX_AGENT);
    }
    WAIT_L0; BAR; SCHED;
    STAMP(3);
    const int col0 = c * 16 + wv * 2;
    const unsigned lo = (h * 64 + (r & 7) * 4 + (r >> 3)) * 32;
    const int nit = col0 == 0 ? 3 : 2;
    u32x4 nr[2][2], ni[2][2];
#define LOADR(rp) do { _Pragma("unroll") for (int b = 0; b < 2; ++b) _Pragma("unroll") for (int ks = 0; ks < 2; ++ks) { \
      nr[b][ks] = ld_sc1(rs, (rp) + ks * 4096 + b * 1024); ni[b][ks] = ld_sc1(rs, ((rp) | 16) + ks * 4096 + b * 1024); } } while (0)
    LOADR(WS_RB + col0 * 8192 + lo);
#pragma unroll
    for (int b = 0; b < 2; ++b)
#pragma unroll
      for (int ks = 0; ks < 2; ++ks) { PIN(nr[b][ks]); PIN(ni[b][ks]); }
#pragma unroll 1
    for (int it = 0; it < nit; ++it) {
      const int col = it == 2 ? 0 : col0 + it, zcol = it == 2 ? 512 : col;
      const bool real = col == 0;
      bf16x8 pr[2][2], pi[2][2];
#pragma unroll
      for (int b = 0; b < 2; ++b)
#pragma unroll
        for (int ks = 0; ks < 2; ++ks) {
          pr[b][ks] = __builtin_bit_cast(bf16x8, nr[b][ks]);
          pi[b][ks] = __builtin_bit_cast(bf16x8, real ? (u32x4){0, 0, 0, 0} : ni[b][ks]);
        }
      FENCE_C;
      LOADR(it == 0 ? WS_RB + (col0 + 1) * 8192 + lo : WS_RB + lo + 16);
      if (it == 0) {
        const __amdgpu_buffer_rsrc_t rD = MKRSRC(wd, F_ * D_ * 2);
        const unsigned lb = (unsigned)(size_t)smem + wv * 1024;
#pragma unroll
        for (int i = 0; i < 28; ++i) DMA16(rD, tid * 16, c * 229376 + i * 8192, lb, 147712);
      }
      f32x16 ar, ai, br, bi;
      cmm<true, false>(ar, ai, pr[0], pi[0], e0);
      cmm<true, false>(br, bi, pr[1], pi[1], e1);
#pragma unroll
      for (int i = 0; i < 16; ++i) {
        const float sr = ar[i] + br[i], si = ai[i] + bi[i], dr = ar[i] - br[i], di = ai[i] - bi[i];
        ar[i] = sr * twc[i] + si * tws[i]; ai[i] = si * twc[i] - sr * tws[i];
        br[i] = dr * twc[i] + di * tws[i]; bi[i] = di * twc[i] - dr * tws[i];
      }
      PACK4();
      cmm<false, false>(ar, ai, pr[0], pi[0], e0);
      cmm<false, false>(br, bi, pr[1], pi[1], e1t);
      {
        LAS const unsigned char* wp = smem + (it == 2 ? WI_NY : wv * WI_STR) + lane * 16;
        const int pstr = it == 2 ? 8192 : 8 * WI_STR;
        const unsigned sel = it == 1 ? 0x07060c0cu : 0x05040c0cu;
#pragma unroll
        for (int g = 0; g < 4; ++g) {
          const u32x4 r0 = *(LAS const u32x4*)(wp + g * 1024), i0 = *(LAS const u32x4*)(wp + pstr + g * 1024);
          const u32x4 r1 = *(LAS const u32x4*)(wp + 4096 + g * 1024), i1 = *(LAS const u32x4*)(wp + pstr + 4096 + g * 1024);
#pragma unroll
          for (int n = 0; n < 4; ++n) {
            const int i = 4 * g + n;
            const float w0r = __uint_as_float(__builtin_amdgcn_perm(r0[n], 0u, sel)), w0i = __uint_as_float(__builtin_amdgcn_perm(i0[n], 0u, sel));
            const float w1r = __uint_as_float(__builtin_amdgcn_perm(r1[n], 0u, sel)), w1i = __uint_as_float(__builtin_amdgcn_perm(i1[n], 0u, sel));
            const float t0 = ar[i], t1 = br[i];
            ar[i] = t0 * w0r - ai[i] * w0i; ai[i] = t0 * w0i + ai[i] * w0r;
            br[i] = t1 * w1r - bi[i] * w1i; bi[i] = t1 * w1i + bi[i] * w1r;
          }
        }
      }
      PACK4();
      cmm<true, true>(ar, ai, pr[0], pi[0], e0);
      cmm<true, true>(br, bi, pr[1], pi[1], e1);
#pragma unroll
      for (int i = 0; i < 16; ++i) {
        const float sr = ar[i] + br[i], si = ai[i] + bi[i], dr = ar[i] - br[i], di = ai[i] - bi[i];
        ar[i] = sr * twc[i] - si * tws[i]; ai[i] = si * twc[i] + sr * tws[i];
        br[i] = dr * twc[i] - di * tws[i]; bi[i] = di * twc[i] + dr * tws[i];
      }
      PACK4();
      cmm<false, true>(ar, ai, pr[0], pi[0], e0);
      cmm<false, true>(br, bi, pr[1], pi[1], e1t);
#pragma unroll
      for (int b = 0; b < 2; ++b)
#pragma unroll
        for (int ks = 0; ks < 2; ++ks) { PIN(nr[b][ks]); PIN(ni[b][ks]); }
      FENCE_C;
      const float sc = real ? 0.5f : 1.f;
      const unsigned zp = WS_Z + zcol * 8192 + lane * 16;
#pragma unroll
      for (int g = 0; g < 4; ++g) {
        u32x4 o0, o1;
#pragma unroll
        for (int n = 0; n < 4; ++n) { o0[n] = pack2(ar[4 * g + n] * sc, ai[4 * g + n]); o1[n] = pack2(br[4 * g + n] * sc, bi[4 * g + n]); }
        st_sc1(rs, zp + g * 2048, o0); st_sc1(rs, zp + g * 2048 + 1024, o1);
      }
    }
  }
  STAMP(4);
  WAIT_V(0); BAR;
  if (tid == 0) __hip_atomic_store((gu64*)(ws + WS_FZ) + c, ep, RLX_AGENT);
  {
    int lane_o = lane; PIN(lane_o);
    const int r = lane_o & 31, h = lane_o >> 5;
    const int g = c >> 3, bz = (c >> 2) & 1, hz = (c >> 1) & 1;
    u32x4 xx[2][4][2];
#pragma unroll
    for (int o = 0; o < 2; ++o)
#pragma unroll
      for (int n = 0; n < 4; ++n) {
        const int tok = 64 * (8 * g + 4 * hz + n) + 32 * bz + (c & 1) * 16 + wv + 8 * o;
        const u16* px = x + (size_t)tok * D_ + r * 32 + h * 16;
        xx[o][n][0] = *(const u32x4*)px; xx[o][n][1] = *(const u32x4*)(px + 8);
      }
    const int b = 16 * ((r >> 2) & 1) + (r & 3) + 4 * (r >> 3);
    bf16x8 b1[2], b2[2], ec[2], ens[2]; float twc[16], tws[16];
#pragma unroll
    for (int ks = 0; ks < 2; ++ks) {
      float p[8], m[8], cc[8], nn[8];
#pragma unroll
      for (int e = 0; e < 8; ++e) {
        const int kb = 8 * h + 4 * ks + (e >> 1);
        const float t = (float)((kb * b) & 31) * (1.f / 32.f);
        const float c1 = cosr(t), s1 = sinr(t);
        p[e] = (e & 1) ? -s1 : c1; m[e] = (e & 1) ? c1 : s1;
        const float t2 = (float)((krow(ks, h, e) * r) & 31) * (1.f / 32.f);
        cc[e] = cosr(t2); nn[e] = -sinr(t2);
      }
      b1[ks] = mk8(pack2(p[0], p[1]), pack2(p[2], p[3]), pack2(p[4], p[5]), pack2(p[6], p[7]));
      b2[ks] = mk8(pack2(m[0], m[1]), pack2(m[2], m[3]), pack2(m[4], m[5]), pack2(m[6], m[7]));
      ec[ks] = mk8(pack2(cc[0], cc[1]), pack2(cc[2], cc[3]), pack2(cc[4], cc[5]), pack2(cc[6], cc[7]));
      ens[ks] = mk8(pack2(nn[0], nn[1]), pack2(nn[2], nn[3]), pack2(nn[4], nn[5]), pack2(nn[6], nn[7]));
    }
#pragma unroll
    for (int i = 0; i < 16; ++i) { const float t = (float)(crow(i, h) * b) * (1.f / 1024.f); twc[i] = cosr(t); tws[i] = sinr(t); }
#pragma unroll
    for (int ks = 0; ks < 2; ++ks) { PIN(b1[ks]); PIN(b2[ks]); PIN(ec[ks]); PIN(ens[ks]); }
    PIN16(twc); PIN16(tws);
    if (wv == 0) poll_flags<0>(ws, WS_FZ, ep);
    BAR; SCHED;
    STAMP(5);
    u32x4 nyq[2];
    {
      u32x4 t[16];
#pragma unroll
      for (int i = 0; i < 16; ++i) t[i] = ld_sc1(rs, WS_Z + ((wv * 16 + i) * 4 + (lane >> 4)) * 8192 + c * 256 + (lane & 15) * 16);
#pragma unroll
      for (int o = 0; o < 2; ++o) nyq[o] = ld_sc1(rs, WS_Z + 512 * 8192 + c * 256 + (wv + 8 * o) * 16);
#pragma unroll
      for (int i = 0; i < 16; ++i) { const int k2 = (wv * 16 + i) * 4 + (lane >> 4); *(LAS u32x4*)(smem + k2 * 256 + (((lane & 15) ^ (k2 & 15)) * 16)) = t[i]; }
    }
    WAIT_L0; BAR; SCHED;
    STAMP(6);
#pragma unroll
    for (int o = 0; o < 2; ++o) {
      const int ro = wv + 8 * o;
      u32x4 z[2][4];
#pragma unroll
      for (int ks = 0; ks < 2; ++ks)
#pragma unroll
        for (int u = 0; u < 4; ++u) z[ks][u] = *(LAS const u32x4*)(smem + (r + 32 * (8 * h + 4 * ks + u)) * 256 + ((ro ^ (r & 15)) * 16));
#pragma unroll
      for (int n = 0; n < 4; ++n) {
        f32x16 ur = zero16(), ui = zero16();
#pragma unroll
        for (int ks = 0; ks < 2; ++ks) { const bf16x8 za = mk8(z[ks][0][n], z[ks][1][n], z[ks][2][n], z[ks][3][n]); ur = MFMA32(za, b1[ks], ur); ui = MFMA32(za, b2[ks], ui); }
        const float ny = bf_lo(nyq[o][n]);
        f32x16 vr, vi;
#pragma unroll
        for (int i = 0; i < 16; ++i) { vr[i] = ur[i] * twc[i] - ui[i] * tws[i]; vi[i] = ui[i] * twc[i] + ur[i] * tws[i]; }
        f32x16 y;
#pragma unroll
        for (int i = 0; i < 16; ++i) y[i] = (i & 1) ? -ny : ny;
#pragma unroll
        for (int ks = 0; ks < 2; ++ks) { y = MFMA32(pack_step(vr, ks), ec[ks], y); y = MFMA32(pack_step(vi, ks), ens[ks], y); }
        u32x4 ov[2];
#pragma unroll
        for (int i = 0; i < 8; ++i) {
          const unsigned yb = pack2(y[2 * i], y[2 * i + 1]), xb = xx[o][n][i >> 2][i & 3];
          ov[i >> 2][i & 3] = pack2(bf_lo(xb) + bf_lo(yb), bf_hi(xb) + bf_hi(yb));
        }
        const int tok = 64 * (8 * g + 4 * hz + n) + 32 * bz + (c & 1) * 16 + ro;
        u16* po = (u16*)(ws + WS_BASE) + (size_t)tok * D_ + r * 32 + h * 16;
        *(u32x4*)po = ov[0]; *(u32x4*)(po + 8) = ov[1];
      }
    }
  }
}

DI float geglu(float g, float u) {
  const float k1 = -2.f * 0.7978845608028654f * 1.4426950408889634f, k2 = k1 * 0.044715f;
  const float e = __builtin_amdgcn_exp2f(g * (k1 + k2 * g * g));
  return g * u * __builtin_amdgcn_rcpf(1.f + e);
}
#define DOT4(acc, v, tagstr) do { const u32x4 u_ = __builtin_bit_cast(u32x4, v); \
    asm volatile("v_dot2c_f32_bf16 %0, %1, %2\n\tv_dot2c_f32_bf16 %0, %1, %3\n\tv_dot2c_f32_bf16 %0, %1, %4\n\tv_dot2c_f32_bf16 %0, %1, %5 ; " tagstr \
                 : "+v"(acc) : "s"(0x3f803f80u), "v"(u_[0]), "v"(u_[1]), "v"(u_[2]), "v"(u_[3])); } while (0)
DI void gemm1_group(LAS unsigned char* lds, const u16* __restrict__ x, const u16* __restrict__ wg, const u16* __restrict__ wu, unsigned char* __restrict__ ws, __amdgpu_buffer_rsrc_t rs STAMP_ARG) {
  const int tid = threadIdx.x, wid = __builtin_amdgcn_readfirstlane(tid >> 6), lane = tid & 63, wr = wid >> 2, wc = wid & 3, fr = lane & 15, fq = lane >> 4;
  const int w = blockIdx.x, xcd = w & 7, idx = w >> 3;
  const unsigned long long e_in = __hip_atomic_load((gu64*)(ws + WS_CTL), RLX_AGENT);
  const int srow = (w & 63) + 64 * krow(w >> 7, (w >> 6) & 1, wid);
  const u32x4 x0 = *(const u32x4*)(x + (size_t)srow * D_ + lane * 8), x1 = *(const u32x4*)(x + (size_t)srow * D_ + 512 + lane * 8);
  SCHED;
  const int pm = (xcd & 1) * 4 + (idx & 3), pn = (xcd >> 1) * 7 + (idx >> 2);
  unsigned voffA[2], voffB[2];
#pragma unroll
  for (int i = 0; i < 2; ++i) {
    const int b = tid * 16 + i * 8192;
    const int ra = b >> 7, ca = ((b >> 4) & 7) ^ ((ra >> 1) & 7);
    voffA[i] = (unsigned)(ra * D_ + ca * 8) * 2u;
    const int kb = b >> 8, p = (b >> 4) & 15, u = (p >> 1) ^ ((kb & 3) | (((kb >> 3) & 1) << 2));
    voffB[i] = (unsigned)(kb * F_ + u * 16 + (p & 1) * 8) * 2u;
  }
  const unsigned ldsw = (unsigned)(size_t)lds + (unsigned)wid * 1024u;
  const __amdgpu_buffer_rsrc_t rA = MKRSRC(x, S_ * D_ * 2), rG = MKRSRC(wg, D_ * F_ * 2), rU = MKRSRC(wu, D_ * F_ * 2);
  const unsigned cA = pm * 256 * D_ * 2, cB = pn * 128 * 2;
  constexpr unsigned hA = 128 * D_ * 2, kA = 128, kB = 64 * F_ * 2;
#define SA(b, h) (((b) * 2 + (h)) * HTB)
#define SB(b, h) ((4 + (b) * 2 + (h)) * HTB)
#define STAGE(bufoff, rsrc, soff, voff) do { DMA16(rsrc, (voff)[0], soff, ldsw, (bufoff)); DMA16(rsrc, (voff)[1], soff, ldsw, (bufoff) + 8192); } while (0)
  unsigned long long ep;
  row_fwd([&] {
    STAGE(SB(0, 0), rG, cB, voffB); STAGE(SA(0, 0), rA, cA, voffA); STAGE(SB(0, 1), rU, cB, voffB); STAGE(SA(0, 1), rA, cA + hA, voffA);
    STAGE(SB(1, 0), rG, cB + kB, voffB); STAGE(SA(1, 0), rA, cA + kA, voffA); STAGE(SB(1, 1), rU, cB + kB, voffB);
  }, lds + K1_SCR, x0, x1, ws, rs, e_in, ep, w, srow STAMP_PASS);
  const unsigned tag = (unsigned)(ep >> 32);
  STAMP(1);
  const int aoff0 = (wr * 64 + fr) * 128 + ((fq ^ (fr >> 1)) * 16), aoff1 = aoff0 ^ 64;
  const int q4 = (lane & 15) >> 2, pp = lane & 3;
  const int boff0 = (8 * fq + q4) * 256 + (((wc * 2) ^ (q4 | ((fq & 1) << 2))) * 32) + pp * 8, boff1 = boff0 ^ 32;
#define LDA(dst, b, h) do { _Pragma("unroll") for (int m = 0; m < 4; ++m) { dst[m][0] = *(const LAS bf16x8*)(lds + SA(b, h) + aoff0 + m * 2048); dst[m][1] = *(const LAS bf16x8*)(lds + SA(b, h) + aoff1 + m * 2048); } } while (0)
#define LDB(dst, b, h) do { _Pragma("unroll") for (int k = 0; k < 2; ++k) { \
    dst[0][k] = cat4(tr_read(lds + SB(b, h) + boff0 + k * 8192), tr_read(lds + SB(b, h) + boff0 + k * 8192 + 1024)); \
    dst[1][k] = cat4(tr_read(lds + SB(b, h) + boff1 + k * 8192), tr_read(lds + SB(b, h) + boff1 + k * 8192 + 1024)); } } while (0)
#define MMA(ai, bj, At, Bt) do { __builtin_amdgcn_s_setprio(1); _Pragma("unroll") for (int m = 0; m < 4; ++m) _Pragma("unroll") for (int n = 0; n < 2; ++n) _Pragma("unroll") for (int k = 0; k < 2; ++k) \
    acc[ai][bj][m][n] = MFMA16(Bt[n][k], At[m][k], acc[ai][bj][m][n]); __builtin_amdgcn_s_setprio(0); } while (0)
#define CSUM do { if (wr == 0) { DOT4(cs0, B0[0][0], "g"); DOT4(cs1, B0[1][0], "g"); DOT4(cs0, B0[0][1], "g\n\ts_nop 2"); DOT4(cs1, B0[1][1], "g\n\ts_nop 2"); } \
    else { DOT4(cs0, B1[0][0], "u"); DOT4(cs1, B1[1][0], "u"); DOT4(cs0, B1[0][1], "u\n\ts_nop 2"); DOT4(cs1, B1[1][1], "u\n\ts_nop 2"); } } while (0)
  f32x4 acc[2][2][4][2];
#pragma unroll
  for (int a = 0; a < 2; ++a)
#pragma unroll
    for (int b = 0; b < 2; ++b)
#pragma unroll
      for (int m = 0; m < 4; ++m)
#pragma unroll
        for (int n = 0; n < 2; ++n) acc[a][b][m][n] = (f32x4){0.f, 0.f, 0.f, 0.f};
  float cs0 = 0.f, cs1 = 0.f;
  bf16x8 At[4][2], B0[2][2], B1[2][2];
  constexpr int nt = D_ / 64;
  WAIT_V(8); BAR;
  if (wr == 1) BAR;
  STAMP(2);
  for (int t = 0; t < nt - 2; t += 2) {
    const unsigned a1 = cA + (t + 1) * kA, a2 = a1 + kA, a3 = a2 + kA, b2 = cB + (t + 2) * kB, b3 = b2 + kB;
    LDB(B0, 0, 0); SCHED; LDA(At, 0, 0); STAGE(SA(1, 1), rA, a1 + hA, voffA);
    WAIT_L(8); BAR; WAIT_L(0); MMA(0, 0, At, B0); BAR; SCHED;
    LDB(B1, 0, 1); STAGE(SB(0, 0), rG, b2, voffB);
    BAR; WAIT_L(0); MMA(0, 1, At, B1); BAR;
    LDA(At, 0, 1); STAGE(SA(0, 0), rA, a2, voffA);
    BAR; WAIT_L(0); MMA(1, 0, At, B0); BAR; SCHED;
    STAGE(SB(0, 1), rU, b2, voffB); CSUM;
    WAIT_V(6); BAR; MMA(1, 1, At, B1); BAR;
    if (t == 0 && tid == 0) __hip_atomic_store((gu64*)(ws + WS_FA) + w, ep, RLX_AGENT);
    LDB(B0, 1, 0); SCHED; LDA(At, 1, 0); STAGE(SA(0, 1), rA, a2 + hA, voffA);
    WAIT_L(8); BAR; WAIT_L(0); MMA(0, 0, At, B0); BAR; SCHED;
    LDB(B1, 1, 1); STAGE(SB(1, 0), rG, b3, voffB);
    BAR; WAIT_L(0); MMA(0, 1, At, B1); BAR;
    LDA(At, 1, 1); STAGE(SA(1, 0), rA, a3, voffA);
    BAR; WAIT_L(0); MMA(1, 0, At, B0); BAR; SCHED;
    STAGE(SB(1, 1), rU, b3, voffB); CSUM;
    WAIT_V(6); BAR; MMA(1, 1, At, B1); BAR;
  }
  STAMP(3);
  unsigned long long st[2];
  gu64* stp = (gu64*)(ws + WS_STAT) + pm * 256 + wr * 64 + lane;
  { LDB(B0, 0, 0); LDA(At, 0, 0); STAGE(SA(1, 1), rA, cA + (nt - 1) * kA + hA, voffA);
    st[0] = __hip_atomic_load(stp, RLX_AGENT); st[1] = __hip_atomic_load(stp + 128, RLX_AGENT);
    BAR; WAIT_L(0); MMA(0, 0, At, B0); BAR;
    LDB(B1, 0, 1); BAR; WAIT_L(0); MMA(0, 1, At, B1); BAR;
    LDA(At, 0, 1); CSUM; WAIT_V(4); BAR; WAIT_L(0); MMA(1, 0, At, B0); MMA(1, 1, At, B1); BAR; }
  { LDB(B0, 1, 0); LDA(At, 1, 0); BAR; WAIT_L(0); MMA(0, 0, At, B0); BAR;
    LDB(B1, 1, 1); WAIT_V(0); BAR; WAIT_L(0); MMA(0, 1, At, B1); BAR;
    LDA(At, 1, 1); CSUM;
    cs0 += __shfl_xor(cs0, 16); cs1 += __shfl_xor(cs1, 16); cs0 += __shfl_xor(cs0, 32); cs1 += __shfl_xor(cs1, 32);
    if (lane < 32) *(LAS float*)(lds + K1_SCR + (wr * 128 + wc * 32 + lane) * 4) = lane < 16 ? cs0 : cs1;
    WAIT_L(0); BAR; MMA(1, 0, At, B0); MMA(1, 1, At, B1); BAR; }
  if (wr == 0) BAR;
  STAMP(4);
#pragma unroll 1
  for (unsigned spins = 0; !__all((unsigned)(st[0] >> 32) == tag && (unsigned)(st[1] >> 32) == tag) && spins < SPIN_CAP; ++spins) {
    __builtin_amdgcn_s_sleep(2);
    st[0] = __hip_atomic_load(stp, RLX_AGENT); st[1] = __hip_atomic_load(stp + 128, RLX_AGENT);
  }
  f32x4 cg[2], cu[2];
#pragma unroll
  for (int n = 0; n < 2; ++n) { cg[n] = *(LAS const f32x4*)(lds + K1_SCR + (wc * 32 + n * 16 + fq * 4) * 4); cu[n] = *(LAS const f32x4*)(lds + K1_SCR + (128 + wc * 32 + n * 16 + fq * 4) * 4); }
#pragma unroll
  for (int ai = 0; ai < 2; ++ai)
#pragma unroll
    for (int m = 0; m < 4; ++m) {
      const unsigned sv = __shfl((unsigned)st[ai], m * 16 + fr);
      const float nm = -bf_lo(sv), rd = __builtin_amdgcn_rcpf(bf_hi(sv));
      u32x4 o;
#pragma unroll
      for (int n = 0; n < 2; ++n) {
        float hv[4];
#pragma unroll
        for (int i = 0; i < 4; ++i) hv[i] = geglu((acc[ai][0][m][n][i] + nm * cg[n][i]) * rd, (acc[ai][1][m][n][i] + nm * cu[n][i]) * rd);
        o[2 * n] = pack2(hv[0], hv[1]); o[2 * n + 1] = pack2(hv[2], hv[3]);
      }
      *(u32x4*)(ws + WS_H + ((size_t)((pm * 2 + ai) * 112 + pn * 4 + wc) * 8 + wr * 4 + m) * 1024 + lane * 16) = o;
    }
  STAMP(5);
#undef SA
#undef SB
#undef STAGE
#undef LDA
#undef LDB
#undef MMA
}
__global__ __launch_bounds__(512, 2) void k1_main(const u16* __restrict__ x, const u16* __restrict__ w_r, const u16* __restrict__ w_i, const u16* __restrict__ wg,
                                                  const u16* __restrict__ wu, const u16* __restrict__ wd, unsigned char* __restrict__ ws) {
  extern __shared__ __attribute__((aligned(16))) unsigned char smem_[];
  LAS unsigned char* lds = (LAS unsigned char*)smem_;
  STAMP_DECL; STAMP(0);
  const __amdgpu_buffer_rsrc_t rs = __builtin_amdgcn_make_buffer_rsrc((void*)ws, 0, WS_USED, 0x00020000);
  if (blockIdx.x < 224) {
    gemm1_group(lds, x, wg, wu, ws, rs STAMP_PASS);
    STAMP_FLUSH(0);
  } else {
    fft_group(lds, x, w_r, w_i, wd, ws, rs STAMP_PASS);
    STAMP_FLUSH(1);
  }
}

constexpr int G2_SLOT = 24576, G2_NS = 6, G2_NT = F_ / 64;
__global__ __launch_bounds__(512, 2) void k2_gemm2(const u16* __restrict__ wd, const unsigned char* __restrict__ ws, u16* __restrict__ out) {
  extern __shared__ __attribute__((aligned(16))) unsigned char smem_[];
  LAS unsigned char* lds = (LAS unsigned char*)smem_;
  const int tid = threadIdx.x, wid = __builtin_amdgcn_readfirstlane(tid >> 6), lane = tid & 63;
  const int xcd = blockIdx.x & 7, idx = blockIdx.x >> 3;
  const int tm = (xcd >> 1) * 4 + (idx & 3), tn = (xcd & 1) * 8 + (idx >> 2);
  STAMP_DECL; STAMP(0);
  const int tok = tid >> 2, dq = tid & 3;
  const size_t go = (size_t)(tm * 128 + tok) * D_ + tn * 64 + dq * 16;
  u32x4 b0, b1;
  if (wid >= 4) {
    const int lw = wid - 4, lt = tid - 256;
    const __amdgpu_buffer_rsrc_t rH = MKRSRC(ws + WS_H, S_ * F_ * 2), rD = MKRSRC(wd, F_ * D_ * 2);
    const int wrow = lt >> 3, wp = lt & 7;
    const unsigned hv = lt * 16, wvo = (wrow * D_ + (((wp >> 1) ^ ((wrow >> 1) & 3)) * 16) + (wp & 1) * 8) * 2;
    const unsigned hs = tm * 112 * 8192, wso = tn * 128;
    const unsigned ldsw = (unsigned)(size_t)lds + lw * 1024u;
#define STAGE2(t, slot) do { DMA16(rD, wvo, wso + (t) * (64 * D_ * 2), ldsw, (slot) * G2_SLOT + 16384); DMA16(rD, wvo, wso + (t) * (64 * D_ * 2) + 32 * D_ * 2, ldsw, (slot) * G2_SLOT + 16384 + 4096); \
      DMA16(rH, hv, hs + (t) * 16384, ldsw, (slot) * G2_SLOT); DMA16(rH, hv, hs + (t) * 16384 + 4096, ldsw, (slot) * G2_SLOT + 4096); \
      DMA16(rH, hv, hs + (t) * 16384 + 8192, ldsw, (slot) * G2_SLOT + 8192); DMA16(rH, hv, hs + (t) * 16384 + 12288, ldsw, (slot) * G2_SLOT + 12288); } while (0)
    STAGE2(0, 0); STAGE2(1, 1); STAGE2(2, 2); STAGE2(3, 3); STAGE2(4, 4); STAGE2(5, 5);
    WAIT_V(30); BAR;
    WAIT_V(24); BAR;
    for (int t = 0; t < 48; t += 6) {
      STAGE2(t + 6, 0); WAIT_V(24); BAR;
      STAGE2(t + 7, 1); WAIT_V(24); BAR;
      STAGE2(t + 8, 2); WAIT_V(24); BAR;
      STAGE2(t + 9, 3); WAIT_V(24); BAR;
      STAGE2(t + 10, 4); WAIT_V(24); BAR;
      STAGE2(t + 11, 5); WAIT_V(24); BAR;
    }
    STAGE2(54, 0); WAIT_V(24); BAR;
    STAGE2(55, 1); WAIT_V(24); BAR;
    WAIT_V(18); BAR;
    WAIT_V(12); BAR;
    WAIT_V(6); BAR;
    WAIT_V(0); BAR;
    b0 = *(const u32x4*)((const u16*)(ws + WS_BASE) + go); b1 = *(const u32x4*)((const u16*)(ws + WS_BASE) + go + 8);
    BAR;
  } else {
    const int wm = wid >> 1, wn = wid & 1, fq = lane >> 4, fr = lane & 15, q4 = (lane & 15) >> 2, pp = lane & 3;
    const int hoff = wm * 4096 + lane * 16;
    const int woff0 = 16384 + (4 * fq + q4) * 128 + (((wn * 2) ^ ((2 * fq + (q4 >> 1)) & 3)) * 32) + pp * 8, woff1 = woff0 ^ 32;
#define LDH(H, slot, k) do { _Pragma("unroll") for (int m = 0; m < 4; ++m) H[k][m] = *(const LAS bf16x8*)(lds + (slot) * G2_SLOT + (k) * 8192 + hoff + m * 1024); } while (0)
#define LDW(W, slot, k) do { W[k][0] = cat4(tr_read(lds + (slot) * G2_SLOT + (k) * 4096 + woff0), tr_read(lds + (slot) * G2_SLOT + (k) * 4096 + woff0 + 2048)); \
      W[k][1] = cat4(tr_read(lds + (slot) * G2_SLOT + (k) * 4096 + woff1), tr_read(lds + (slot) * G2_SLOT + (k) * 4096 + woff1 + 2048)); } while (0)
#define MM2(H, W, k) do { _Pragma("unroll") for (int m = 0; m < 4; ++m) _Pragma("unroll") for (int n = 0; n < 2; ++n) acc[m][n] = MFMA16(W[k][n], H[k][m], acc[m][n]); } while (0)
    f32x4 acc[4][2];
#pragma unroll
    for (int m = 0; m < 4; ++m) { acc[m][0] = (f32x4){0.f, 0.f, 0.f, 0.f}; acc[m][1] = (f32x4){0.f, 0.f, 0.f, 0.f}; }
    bf16x8 Ha[2][4], Wa[2][2], Hb[2][4], Wb[2][2];
    BAR;
    LDW(Wa, 0, 0); LDH(Ha, 0, 0); LDW(Wa, 0, 1); LDH(Ha, 0, 1);
    WAIT_L0; BAR; SCHED;
    STAMP(1);
#define ITER(nslot, Hc, Wc, Hn, Wn) do { LDW(Wn, nslot, 0); LDH(Hn, nslot, 0); SCHED; MM2(Hc, Wc, 0); SCHED; LDW(Wn, nslot, 1); LDH(Hn, nslot, 1); SCHED; MM2(Hc, Wc, 1); SCHED; WAIT_L0; BAR; SCHED; } while (0)
    for (int t = 0; t < 54; t += 6) {
      ITER(1, Ha, Wa, Hb, Wb);
      ITER(2, Hb, Wb, Ha, Wa);
      ITER(3, Ha, Wa, Hb, Wb);
      ITER(4, Hb, Wb, Ha, Wa);
      ITER(5, Ha, Wa, Hb, Wb);
      ITER(0, Hb, Wb, Ha, Wa);
    }
    STAMP(2);
    b0 = *(const u32x4*)((const u16*)(ws + WS_BASE) + go); b1 = *(const u32x4*)((const u16*)(ws + WS_BASE) + go + 8);
    ITER(1, Ha, Wa, Hb, Wb);
    MM2(Hb, Wb, 0); MM2(Hb, Wb, 1);
    STAMP(3);
#pragma unroll
    for (int m = 0; m < 4; ++m)
#pragma unroll
      for (int n = 0; n < 2; ++n) *(LAS f32x4*)(lds + (wm * 64 + m * 16 + fr) * 272 + (wn * 32 + n * 16 + fq * 4) * 4) = acc[m][n];
  }
  __syncthreads();
  u32x4 o[2];
#pragma unroll
  for (int i = 0; i < 4; ++i) {
    const f32x4 a = *(const LAS f32x4*)(lds + tok * 272 + (dq * 16 + i * 4) * 4);
    const unsigned p0 = pack2(a[0], a[1]), p1 = pack2(a[2], a[3]);
    const unsigned c0 = (i < 2 ? b0 : b1)[(2 * i) & 3], c1 = (i < 2 ? b0 : b1)[(2 * i + 1) & 3];
    o[i >> 1][(2 * i) & 3] = pack2(bf_lo(c0) + bf_lo(p0), bf_hi(c0) + bf_hi(p0));
    o[i >> 1][(2 * i + 1) & 3] = pack2(bf_lo(c1) + bf_lo(p1), bf_hi(c1) + bf_hi(p1));
  }
  *(u32x4*)(out + go) = o[0]; *(u32x4*)(out + go + 8) = o[1];
  STAMP(4);
  STAMP_FLUSH(4);
}

extern "C" void kernel_launch(void* const* d_in, const int* in_sizes, int n_in, void* d_out, int out_size, void* d_ws, size_t ws_size, hipStream_t stream) {
  const u16* x = (const u16*)d_in[0]; const u16* w_r = (const u16*)d_in[1]; const u16* w_i = (const u16*)d_in[2];
  const u16* wg = (const u16*)d_in[4]; const u16* wu = (const u16*)d_in[5]; const u16* wd = (const u16*)d_in[6];
  unsigned char* ws = (unsigned char*)d_ws;
  (void)hipFuncSetAttribute((const void*)k1_main, hipFuncAttributeMaxDynamicSharedMemorySize, K1_LDS);
  (void)hipFuncSetAttribute((const void*)k2_gemm2, hipFuncAttributeMaxDynamicSharedMemorySize, G2_SLOT * G2_NS);
  k1_main<<<256, 512, K1_LDS, stream>>>(x, w_r, w_i, wg, wu, wd, ws);
  k2_gemm2<<<256, 512, G2_SLOT * G2_NS, stream>>>(wd, ws, (u16*)d_out);
}
```

```cpp
#include <hip/hip_runtime.h>
#include <hip/hip_bf16.h>
#include <stdint.h>

#define LAS __attribute__((address_space(3)))
#define DI __device__ __forceinline__
typedef unsigned short u16;
typedef __bf16 bf16x8 __attribute__((ext_vector_type(8)));
typedef __bf16 bf16x4 __attribute__((ext_vector_type(4)));
typedef __bf16 bf16x2 __attribute__((ext_vector_type(2)));
typedef float f32x2 __attribute__((ext_vector_type(2)));
typedef float f32x4 __attribute__((ext_vector_type(4)));
typedef float f32x16 __attribute__((ext_vector_type(16)));
typedef unsigned u32x2 __attribute__((ext_vector_type(2)));
typedef unsigned u32x4 __attribute__((ext_vector_type(4)));
typedef unsigned u32x4u __attribute__((ext_vector_type(4), aligned(2)));

constexpr int S_ = 2048, D_ = 1024, F_ = 3584, DF_ = 513;
constexpr unsigned WS_CTL = 0;
constexpr unsigned WS_FA = 4096;
constexpr unsigned WS_FZ = 8192;
constexpr unsigned WS_STAT = 16384;
constexpr unsigned WS_NYQ = 32768;
constexpr unsigned WS_CS = 65536;
constexpr unsigned WS_H = 4u << 20;
constexpr unsigned WS_RW = 20u << 20;
constexpr unsigned WS_Z = 28u << 20;
constexpr unsigned WS_Y = 33u << 20;
constexpr unsigned WS_USED = 38u << 20;
typedef __attribute__((address_space(1))) unsigned long long gu64;
typedef __attribute__((address_space(1))) unsigned gu32;
#define RLX_AGENT __ATOMIC_RELAXED, __HIP_MEMORY_SCOPE_AGENT
constexpr unsigned SPIN_CAP = 1u << 16;
constexpr int HTB = 16384, K1_SCR = 131072, K1_LDS = 163840;

DI unsigned pack2(float a, float b) { f32x2 v = {a, b}; return __builtin_bit_cast(unsigned, __builtin_convertvector(v, bf16x2)); }
DI float bf_lo(unsigned u) { return __uint_as_float(u << 16); }
DI float bf_hi(unsigned u) { return __uint_as_float(u & 0xffff0000u); }
DI float rbf(float a) { return bf_lo(pack2(a, a)); }
constexpr int crow(int reg, int h) { return (reg & 3) + 8 * (reg >> 2) + 4 * h; }
constexpr int krow(int s, int h, int j) { return 16 * s + 8 * (j >> 2) + 4 * h + (j & 3); }
struct Trig {
  float f[2048]; unsigned short b[2048];
  constexpr Trig() : f(), b() {
    for (int k = 0; k <= 512; ++k) {
      const bool sw = k > 256;
      const double x = (sw ? 512 - k : k) * (6.283185307179586476925 / 2048), x2 = x * x;
      double t = sw ? x : 1.0, s = t;
      for (int n = sw ? 2 : 1; n < 22; n += 2) { t *= -x2 / (n * (n + 1)); s += t; }
      const float v = (float)s;
      f[k] = v; f[(2048 - k) & 2047] = v; f[1024 - k] = -v; f[1024 + k] = -v;
    }
    f[512] = 0.f; f[1536] = 0.f;
    for (int k = 0; k < 2048; ++k) { const unsigned u = __builtin_bit_cast(unsigned, f[k]); b[k] = (unsigned short)((u + 0x7fffu + ((u >> 16) & 1)) >> 16); }
  }
};
constexpr Trig TG;
struct Tabs {
  unsigned v[48 * 256];
  static constexpr int ang(int m, int ks, int r, int h, int e) {
    const int a = krow(ks, h, e), b = 16 * ((r >> 2) & 1) + (r & 3) + 4 * (r >> 3), kb = 8 * h + 4 * ks + (e >> 1);
    switch (m) {
      case 0: return 64 * a * r;
      case 1: return 64 * a * r + 512;
      case 2: return 64 * a * (r >> 1) + 512 * (r & 1);
      case 3: return 64 * a * (r >> 1) + 512 * (r & 1) - 512;
      case 4: return 64 * a * r - 512;
      case 5: return 32 * (2 * a + 1) * r;
      case 6: return 32 * (2 * a + 1) * r - 512;
      case 7: return 32 * (2 * r + 1) * a;
      case 8: return 32 * (2 * r + 1) * a - 512;
      case 9: return 64 * kb * b + 512 * (e & 1);
      default: return 64 * kb * b + 512 * (e & 1) - 512;
    }
  }
  constexpr void frag(int q, int m) {
    for (int ks = 0; ks < 2; ++ks)
      for (int l = 0; l < 64; ++l)
        for (int d = 0; d < 4; ++d)
          v[(q + ks) * 256 + l * 4 + d] = TG.b[ang(m, ks, l & 31, l >> 5, 2 * d) & 2047] | (unsigned)TG.b[ang(m, ks, l & 31, l >> 5, 2 * d + 1) & 2047] << 16;
  }
  constexpr void tw(int q, int m) {
    for (int l = 0; l < 64; ++l)
      for (int i = 0; i < 16; ++i) {
        const int r = l & 31, a = crow(i, l >> 5) * (m == 0 ? 2 * r : m == 1 ? r : 2 * (16 * ((r >> 2) & 1) + (r & 3) + 4 * (r >> 3)));
        const float sc = m == 1 ? 1.f / 1024.f : 1.f;
        v[(q + (i >> 2)) * 256 + l * 4 + (i & 3)] = __builtin_bit_cast(unsigned, TG.f[a & 2047] * sc);
        v[(q + 4 + (i >> 2)) * 256 + l * 4 + (i & 3)] = __builtin_bit_cast(unsigned, TG.f[(a - 512) & 2047] * sc);
      }
  }
  constexpr Tabs() : v() {
    frag(0, 0); frag(2, 1); frag(4, 2); frag(6, 3); tw(8, 0);
    frag(16, 0); frag(18, 4); frag(20, 5); frag(22, 6); frag(24, 7); frag(26, 8); tw(28, 1);
    frag(36, 9); frag(38, 10); tw(40, 2);
  }
};
static __device__ const __attribute__((aligned(128))) Tabs g_tab;
DI bf16x8 mk8(unsigned a, unsigned b, unsigned c, unsigned d) { u32x4 v = {a, b, c, d}; return __builtin_bit_cast(bf16x8, v); }
DI bf16x8 pack_step(const f32x16& x, int s) { return mk8(pack2(x[8 * s], x[8 * s + 1]), pack2(x[8 * s + 2], x[8 * s + 3]), pack2(x[8 * s + 4], x[8 * s + 5]), pack2(x[8 * s + 6], x[8 * s + 7])); }
DI bf16x4 tr_read(LAS const unsigned char* p) { return __builtin_amdgcn_ds_read_tr16_b64_v4bf16((LAS bf16x4*)p); }
DI bf16x8 cat4(bf16x4 a, bf16x4 b) { return __builtin_shufflevector(a, b, 0, 1, 2, 3, 4, 5, 6, 7); }
#define MFMA32(a, b, c) __builtin_amdgcn_mfma_f32_32x32x16_bf16((a), (b), (c), 0, 0, 0)
#define MFMA16(a, b, c) __builtin_amdgcn_mfma_f32_16x16x32_bf16((a), (b), (c), 0, 0, 0)
#define GLDS16(g, l) __builtin_amdgcn_global_load_lds((const unsigned*)(g), (LAS unsigned*)(l), 16, 0, 0)
#define DMA16P(rsrc, voff, soff, lbase, limm, pol) asm volatile("s_add_i32 m0, %0, %4\n\ts_nop 0\n\tbuffer_load_dwordx4 %1, %2, %3 offen" pol " lds" :: "s"(lbase), "v"(voff), "s"(rsrc), "s"(soff), "i"(limm) : "scc", "memory")
#define DMA16(rsrc, voff, soff, lbase, limm) DMA16P(rsrc, voff, soff, lbase, limm, "")
#define POLX ""
#define POLW " nt"
#define POLA ""
#define POLB ""
#define POL_rA POLA
#define POL_rG POLB
#define POL_rU POLB
#define MKRSRC(p, n) __builtin_amdgcn_make_buffer_rsrc((void*)(p), 0, (n), 0x00020000)
#define FENCE_C asm volatile("" ::: "memory")
#define WAIT_V(n) asm volatile("s_waitcnt vmcnt(" #n ")" ::: "memory")
#define WAIT_L(n) asm volatile("s_waitcnt lgkmcnt(" #n ")" ::: "memory")
#define WAIT_L0 __builtin_amdgcn_s_waitcnt(0xC07F)
#define BAR __builtin_amdgcn_s_barrier()
#define SCHED __builtin_amdgcn_sched_barrier(0)

#define STAMP_DECL
#define STAMP_ARG
#define STAMP_PASS
#define STAMP(s)
#define STAMPB(s)
#define STAMP_FLUSH(k)
#define PIN(v) asm volatile("" : "+v"(v))
#define PIN16(a) do { _Pragma("unroll") for (int i_ = 0; i_ < 16; ++i_) PIN(a[i_]); } while (0)
DI int otid() { int t = threadIdx.x; PIN(t); return t; }
DI f32x16 zero16() { f32x16 z; for (int i = 0; i < 16; ++i) z[i] = 0.f; return z; }
template <int CTRL, int RM> DI float dpp_add(float v) { return v + __builtin_bit_cast(float, __builtin_amdgcn_update_dpp(0, __builtin_bit_cast(int, v), CTRL, RM, 0xf, true)); }
DI float wave_sum(float v) {
  v = dpp_add<0x111, 0xf>(v); v = dpp_add<0x112, 0xf>(v); v = dpp_add<0x114, 0xf>(v); v = dpp_add<0x118, 0xf>(v);
  v = dpp_add<0x142, 0xa>(v); v = dpp_add<0x143, 0xc>(v);
  return __builtin_bit_cast(float, __builtin_amdgcn_readlane(__builtin_bit_cast(int, v), 63));
}
DI u32x4 ld_sc1(__amdgpu_buffer_rsrc_t rs, unsigned off) { return __builtin_amdgcn_raw_buffer_load_b128(rs, off, 0, 16); }
DI void st_sc1(__amdgpu_buffer_rsrc_t rs, unsigned off, u32x4 v) { __builtin_amdgcn_raw_buffer_store_b128(v, rs, off, 0, 16); }
template <int N> DI void poll_flags(const unsigned char* ws, unsigned off, unsigned long long ep) {
  const int lane = threadIdx.x & 63;
  gu64* f = (gu64*)(ws + off) + (N ? lane : (lane & 31));
#pragma unroll 1
  for (unsigned spins = 0; spins < SPIN_CAP; ++spins) {
    bool ok = true;
#pragma unroll
    for (int k = 0; k < (N ? N : 1); ++k) ok &= __hip_atomic_load(f + 64 * k, RLX_AGENT) == ep;
    if (__all(ok)) break;
    __builtin_amdgcn_s_sleep(2);
  }
  __builtin_amdgcn_fence(__ATOMIC_ACQUIRE, "wavefront");
}

DI unsigned long long next_epoch(unsigned long long e_in) {
  const unsigned hi = (unsigned)__builtin_amdgcn_readfirstlane((unsigned)(e_in >> 32)) + 0x9E3779B9u, lo = (unsigned)__builtin_amdgcn_readfirstlane((unsigned)e_in) + 0x7F4A7C15u;
  return ((unsigned long long)hi << 32) | lo;
}
constexpr int L_TROW = 3 * HTB;
#define LDT(base, q) (*(LAS const bf16x8*)((base) + (q) * 1024 + lane * 16))
#define LDTW(dst, base, q) do { _Pragma("unroll") for (int g_ = 0; g_ < 4; ++g_) { const f32x4 t_ = *(LAS const f32x4*)((base) + ((q) + g_) * 1024 + lane * 16); \
    dst[4 * g_] = t_[0]; dst[4 * g_ + 1] = t_[1]; dst[4 * g_ + 2] = t_[2]; dst[4 * g_ + 3] = t_[3]; } } while (0)
template <int N0, int N1, int N2, class F>
DI unsigned long long row_fwd(F&& issue, LAS unsigned char* lds, const int scro, const u16* __restrict__ x, const u16* __restrict__ w_r, const u16* __restrict__ w_i,
                              unsigned char* __restrict__ ws, __amdgpu_buffer_rsrc_t rs STAMP_ARG) {
  const int tid = otid(), lane = tid & 63, j = __builtin_amdgcn_readfirstlane(tid >> 6), w = blockIdx.x;
  const int r = lane & 31, h = lane >> 5;
  const int s = (w & 63) + 64 * krow(w >> 7, (w >> 6) & 1, j), k1 = 256 * (w >> 5) + 64 * (j & 3) + 32 * (j >> 2) + (w & 31);
  u32x4 fr, fi; unsigned nr, ni; unsigned long long e_in;
  {
    const unsigned vo = lane * 16, vz = 0;
    const __amdgpu_buffer_rsrc_t rX = MKRSRC(x, S_ * D_ * 2), rT = MKRSRC(&g_tab, sizeof(Tabs));
    const unsigned lb = (unsigned)(size_t)lds + j * 2048, lx = lb + scro;
    DMA16P(rX, vo, s * 2048, lx, 0, POLX); DMA16P(rX, vo, s * 2048 + 1024, lx, 1024, POLX);
    DMA16(rT, vo, j * 2048, lb, L_TROW); DMA16(rT, vo, j * 2048 + 1024, lb, L_TROW + 1024);
    BAR;
    const u16* pr = w_r + (size_t)k1 * DF_; const u16* pi = w_i + (size_t)k1 * DF_;
    asm volatile("global_load_dwordx2 %0, %6, %9 sc1\n\tglobal_load_dwordx4 %1, %5, %7" POLW "\n\tglobal_load_dwordx4 %2, %5, %8" POLW "\n\tglobal_load_ushort %3, %6, %7 offset:1024" POLW "\n\tglobal_load_ushort %4, %6, %8 offset:1024" POLW
                 : "=&v"(e_in), "=&v"(fr), "=&v"(fi), "=&v"(nr), "=&v"(ni) : "v"(vo), "v"(vz), "s"(pr), "s"(pi), "s"(ws + WS_CTL) : "memory");
  }
  issue(0);
  LAS unsigned char* scr = lds + scro;
  LAS unsigned char* xt = scr + j * 2048;
  LAS const unsigned char* tb = lds + L_TROW;
  const int g2 = (lane >> 4) & 1, qq = (lane & 15) >> 2, pp = lane & 3, swz = (j & 3) << 6, g4 = lane >> 4;
  int xo = (4 * h + qq) * 64 + g2 * 32 + pp * 8, wo = 16384 + ((lane * 32) ^ swz), b0 = (r * 4 + h * 256) ^ swz;
  int to = qq * 2048 + ((j * 256 + g4 * 32 + pp * 8) ^ (qq << 6));
  unsigned so = WS_RW + w * 32768 + (j * 8 + g4) * 512 + (lane & 15) * 16;
  float sgn = (lane & 1) ? -1.f : 1.f, dc = r == 0 ? -32.f : 0.f;
  PIN(xo); PIN(wo); PIN(b0); PIN(to); PIN(so); PIN(sgn); PIN(dc);
  STAMPB(1);
  asm volatile("s_waitcnt vmcnt(%0)" :: "n"(N0 + 5) : "memory");
  BAR; FENCE_C;
  STAMPB(2);
  bf16x8 xa[2];
#pragma unroll
  for (int ks = 0; ks < 2; ++ks) xa[ks] = cat4(tr_read(xt + xo + ks * 1024), tr_read(xt + xo + ks * 1024 + 512));
  f32x16 tr = zero16(), ti = zero16();
#pragma unroll
  for (int ks = 0; ks < 2; ++ks) { tr = MFMA32(xa[ks], LDT(tb, ks), tr); ti = MFMA32(xa[ks], LDT(tb, 2 + ks), ti); }
  float twc[16], tws[16];
  LDTW(twc, tb, 8); LDTW(tws, tb, 12);
  bf16x8 a1[2], a2[2];
#pragma unroll
  for (int ks = 0; ks < 2; ++ks) { a1[ks] = LDT(tb, 4 + ks); a2[ks] = LDT(tb, 6 + ks); }
  SCHED; issue(1); SCHED;
  f32x2 v[8];
#pragma unroll
  for (int i = 0; i < 8; ++i) { const unsigned u = __builtin_bit_cast(u32x4, xa[i >> 2])[i & 3]; v[i] = (f32x2){bf_lo(u), bf_hi(u)}; }
  const f32x2 s2 = ((v[0] + v[1]) + (v[2] + v[3])) + ((v[4] + v[5]) + (v[6] + v[7]));
  const float ls = s2[0] + s2[1];
  const float mean = wave_sum(ls) * (1.f / 1024.f), alt = wave_sum(ls * sgn);
  const float mb = rbf(mean), dm = dc * mb;
  f32x16 ur, ui;
#pragma unroll
  for (int i = 0; i < 16; ++i) { ur[i] = __builtin_fmaf(tr[i], twc[i], __builtin_fmaf(ti[i], tws[i], dm)); ui[i] = ti[i] * twc[i] - tr[i] * tws[i]; }
  f32x16 o = zero16();
#pragma unroll
  for (int ks = 0; ks < 2; ++ks) { bf16x8 pr = pack_step(ur, ks), pi = pack_step(ui, ks); PIN(pr); PIN(pi); o = MFMA32(a1[ks], pr, o); o = MFMA32(a2[ks], pi, o); }
  SCHED; issue(2); SCHED;
  f32x2 q0 = {0.f, 0.f}, q1 = {0.f, 0.f};
#pragma unroll
  for (int i = 0; i < 4; ++i) { const f32x2 d0 = v[2 * i] - mean, d1 = v[2 * i + 1] - mean; q0 += d0 * d0; q1 += d1 * d1; }
  q0 += q1;
  const float var = wave_sum(q0[0] + q0[1]) * (1.f / 1024.f);
  const float den = rbf(__builtin_amdgcn_sqrtf(rbf(rbf(var) + 1e-5f))), rden = __builtin_amdgcn_rcpf(den);
  asm volatile("s_waitcnt vmcnt(%5)" : "+v"(e_in), "+v"(fr), "+v"(fi), "+v"(nr), "+v"(ni) : "n"(N0 + N1 + N2) : "memory");
  const unsigned long long ep = next_epoch(e_in);
  {
    u32x4 a, b;
#pragma unroll
    for (int i = 0; i < 2; ++i) {
      a[2 * i] = __builtin_amdgcn_perm(fi[i], fr[i], 0x05040100u); a[2 * i + 1] = __builtin_amdgcn_perm(fi[i], fr[i], 0x07060302u);
      b[2 * i] = __builtin_amdgcn_perm(fi[2 + i], fr[2 + i], 0x05040100u); b[2 * i + 1] = __builtin_amdgcn_perm(fi[2 + i], fr[2 + i], 0x07060302u);
    }
    *(LAS u32x4*)(xt + wo) = a; *(LAS u32x4*)(xt + wo + 16) = b;
  }
  if (lane == 0) o[1] = alt;
#pragma unroll
  for (int q = 0; q < 8; ++q) *(LAS unsigned*)(xt + (b0 ^ ((q & 1) * 128)) + (q >> 1) * 512) = pack2(o[2 * q] * rden, o[2 * q + 1] * rden);
  WAIT_L0; BAR; SCHED;
  STAMPB(3);
  bf16x8 c[4];
#pragma unroll
  for (int i = 0; i < 4; ++i) { LAS const unsigned char* p = scr + (i >> 1) * 16384 + (to ^ ((i & 1) * 128)); c[i] = cat4(tr_read(p), tr_read(p + 8192)); }
  if (lane == 0) {
    __hip_atomic_store((gu64*)(ws + WS_STAT) + s, (ep & 0xffffffff00000000ull) | pack2(mb, den), RLX_AGENT);
    __hip_atomic_store((gu32*)(ws + WS_NYQ) + w * 8 + j, nr | ni << 16, RLX_AGENT);
  }
#pragma unroll
  for (int i = 0; i < 4; ++i) st_sc1(rs, so + (i & 1) * 2048 + (i >> 1) * 256, __builtin_bit_cast(u32x4, c[i]));
  return ep;
}

struct CF { bf16x8 c[2], s[2]; };
DI bf16x8 neg8(bf16x8 v) { u32x4 u = __builtin_bit_cast(u32x4, v); u ^= 0x80008000u; return __builtin_bit_cast(bf16x8, u); }
template <bool DA, bool INV>
DI void cmm(f32x16& re, f32x16& im, const bf16x8 (&xr)[2], const bf16x8 (&xi)[2], const CF& f) {
  re = zero16(); im = zero16();
#pragma unroll
  for (int ks = 0; ks < 2; ++ks) {
    const bf16x8 p = INV ? neg8(xi[ks]) : xi[ks], m = INV ? xr[ks] : neg8(xr[ks]);
    if (DA) { re = MFMA32(xr[ks], f.c[ks], re); im = MFMA32(xi[ks], f.c[ks], im); re = MFMA32(p, f.s[ks], re); im = MFMA32(m, f.s[ks], im); }
    else    { re = MFMA32(f.c[ks], xr[ks], re); im = MFMA32(f.c[ks], xi[ks], im); re = MFMA32(f.s[ks], p, re); im = MFMA32(f.s[ks], m, im); }
  }
}
#define PACK4() do { _Pragma("unroll") for (int ks = 0; ks < 2; ++ks) { pr[0][ks] = pack_step(ar, ks); pi[0][ks] = pack_step(ai, ks); pr[1][ks] = pack_step(br, ks); pi[1][ks] = pack_step(bi, ks); } } while (0)

DI void fft_group(LAS unsigned char* smem, const u16* __restrict__ x, const u16* __restrict__ w_r, const u16* __restrict__ w_i,
                  unsigned char* __restrict__ ws, __amdgpu_buffer_rsrc_t rs STAMP_ARG) {
  const int wv = __builtin_amdgcn_readfirstlane(threadIdx.x >> 6);
  const int c = blockIdx.x - 224;
  const unsigned long long ep = row_fwd<4, 0, 0>([&](int q) {
    if (q) return;
    const __amdgpu_buffer_rsrc_t rT = MKRSRC(&g_tab, sizeof(Tabs));
    const unsigned lb = (unsigned)(size_t)smem + wv * 1024, vo = otid() * 16;
#pragma unroll
    for (int i = 0; i < 4; ++i) DMA16(rT, vo, 16384 + i * 8192, lb, K1_SCR + i * 8192);
  }, smem, 65536, x, w_r, w_i, ws, rs STAMP_PASS);
  STAMP(1); STAMPB(4);
  WAIT_V(0); BAR;
  STAMPB(5);
  if (threadIdx.x == 0) __hip_atomic_store((gu64*)(ws + WS_FA) + blockIdx.x, ep, RLX_AGENT);
  LAS const unsigned char* tb = smem + K1_SCR;
  {
    const int lane = otid() & 63, r = lane & 31, h = lane >> 5;
    CF e0, e1, e1t;
#pragma unroll
    for (int ks = 0; ks < 2; ++ks) { e0.c[ks] = LDT(tb, ks); e0.s[ks] = LDT(tb, 2 + ks); e1.c[ks] = LDT(tb, 4 + ks); e1.s[ks] = LDT(tb, 6 + ks); e1t.c[ks] = LDT(tb, 8 + ks); e1t.s[ks] = LDT(tb, 10 + ks); }
    if (wv == 0) {
      poll_flags<4>(ws, WS_FA, ep);
      if (c == 0 && lane == 0) __hip_atomic_store((gu64*)(ws + WS_CTL), ep, RLX_AGENT);
    }
    BAR; FENCE_C;
    STAMP(2);
    u32x4 t1[16];
    LAS unsigned char* sw = smem + (wv * 2 + h) * 512 + ((r ^ (wv * 2 + h)) * 16);
    {
      u32x4 t0[16];
      const unsigned go = WS_RW + (wv * 2 + h) * 32768 + c * 1024 + r * 16;
#pragma unroll
      for (int i = 0; i < 16; ++i) t0[i] = __builtin_amdgcn_raw_buffer_load_b128(rs, go, i * 524288, 16);
#pragma unroll
      for (int i = 0; i < 16; ++i) { *(LAS u32x4*)(sw + i * 8192) = t0[i]; FENCE_C; t1[i] = __builtin_amdgcn_raw_buffer_load_b128(rs, go + 512, i * 524288, 16); FENCE_C; }
    }
    WAIT_L0; BAR; SCHED;
    STAMP(3);
    if (wv < 4) __builtin_amdgcn_s_setprio(2);
    const bool extra = c == 0 && wv == 0;
    const int nit = extra ? 3 : 2;
    LAS unsigned char* ri = smem + (h * 64 + r) * 512 + (((wv * 2) ^ (r & 15)) * 16);
    LAS unsigned char* wi = smem + lane * 512 + 256 + (((wv * 2) ^ (r & 15)) * 16);
#define X16(p) ((LAS unsigned char*)((size_t)(p) ^ 16u))
#pragma unroll 1
    for (int it = 0; it < nit; ++it) {
      const bool real = extra && it != 1;
      bf16x8 pr[2][2], pi[2][2];
#pragma unroll
      for (int b = 0; b < 2; ++b)
#pragma unroll
        for (int ks = 0; ks < 2; ++ks) {
          pr[b][ks] = *(LAS const bf16x8*)(ri + ks * 65536 + b * 16384);
          const u32x4 t = *(LAS const u32x4*)(X16(ri) + ks * 65536 + b * 16384);
          pi[b][ks] = __builtin_bit_cast(bf16x8, real ? (u32x4){0, 0, 0, 0} : t);
        }
      f32x16 ar, ai, br, bi;
      cmm<true, false>(ar, ai, pr[0], pi[0], e0);
      cmm<true, false>(br, bi, pr[1], pi[1], e1);
      {
        float twc[16], tws[16];
        LDTW(twc, tb, 12); LDTW(tws, tb, 16);
#pragma unroll
        for (int i = 0; i < 16; ++i) {
          const float sr = ar[i] + br[i], si = ai[i] + bi[i], dr = ar[i] - br[i], di = ai[i] - bi[i];
          ar[i] = sr * twc[i] + si * tws[i]; ai[i] = si * twc[i] - sr * tws[i];
          br[i] = dr * twc[i] + di * tws[i]; bi[i] = di * twc[i] - dr * tws[i];
        }
      }
      PACK4();
      cmm<false, false>(ar, ai, pr[0], pi[0], e0);
      cmm<false, false>(br, bi, pr[1], pi[1], e1t);
#pragma unroll
      for (int g = 0; g < 4; ++g) {
        const u32x4 wr4 = *(LAS const u32x4*)(wi + g * 32768), wi4 = *(LAS const u32x4*)(X16(wi) + g * 32768);
#pragma unroll
        for (int n = 0; n < 4; ++n) {
          const int i = 4 * g + n;
          const float w0r = (n & 1) ? bf_hi(wr4[n >> 1]) : bf_lo(wr4[n >> 1]), w0i = (n & 1) ? bf_hi(wi4[n >> 1]) : bf_lo(wi4[n >> 1]);
          const float w1r = (n & 1) ? bf_hi(wr4[2 + (n >> 1)]) : bf_lo(wr4[2 + (n >> 1)]), w1i = (n & 1) ? bf_hi(wi4[2 + (n >> 1)]) : bf_lo(wi4[2 + (n >> 1)]);
          const float t0 = ar[i], t1 = br[i];
          ar[i] = t0 * w0r - ai[i] * w0i; ai[i] = t0 * w0i + ai[i] * w0r;
          br[i] = t1 * w1r - bi[i] * w1i; bi[i] = t1 * w1i + bi[i] * w1r;
        }
      }
      PACK4();
#pragma unroll
      for (int q = 0; q < 4; ++q) { PIN(pr[q >> 1][q & 1]); PIN(pi[q >> 1][q & 1]); }
      if (it == 0) {
        WAIT_L0; BAR; FENCE_C;
#pragma unroll
        for (int i = 0; i < 16; ++i) *(LAS u32x4*)(sw + i * 8192) = t1[i];
        if (extra) {
#pragma unroll
          for (int q = 0; q < 4; ++q) t1[q] = ld_sc1(rs, WS_RW + ((q >> 1) * 128 + h * 64 + (q & 1) * 32 + r) * 32768 + 16);
#pragma unroll
          for (int q = 0; q < 8; ++q) t1[4 + q] = ld_sc1(rs, WS_NYQ + (q >> 1) * 2048 + lane * 32 + (q & 1) * 16);
        }
      } else if (extra) {
#pragma unroll
        for (int q = 0; q < 4; ++q) {
          *(LAS u32x4*)(ri + (q >> 1) * 65536 + (q & 1) * 16384) = t1[q];
          u32x4 a, b;
#pragma unroll
          for (int d = 0; d < 4; ++d) {
            const unsigned u0 = t1[4 + 2 * q + (d >> 1)][2 * (d & 1)], u1 = t1[4 + 2 * q + (d >> 1)][2 * (d & 1) + 1];
            a[d] = __builtin_amdgcn_perm(u1, u0, 0x05040100u); b[d] = __builtin_amdgcn_perm(u1, u0, 0x07060302u);
          }
          *(LAS u32x4*)(wi + q * 32768) = a; *(LAS u32x4*)(X16(wi) + q * 32768) = b;
        }
      }
      FENCE_C;
      cmm<true, true>(ar, ai, pr[0], pi[0], e0);
      cmm<true, true>(br, bi, pr[1], pi[1], e1);
      {
        float twc[16], tws[16];
        LDTW(twc, tb, 12); LDTW(tws, tb, 16);
#pragma unroll
        for (int i = 0; i < 16; ++i) {
          const float sr = ar[i] + br[i], si = ai[i] + bi[i], dr = ar[i] - br[i], di = ai[i] - bi[i];
          ar[i] = sr * twc[i] - si * tws[i]; ai[i] = si * twc[i] + sr * tws[i];
          br[i] = dr * twc[i] - di * tws[i]; bi[i] = di * twc[i] + dr * tws[i];
        }
      }
      PACK4();
      cmm<false, true>(ar, ai, pr[0], pi[0], e0);
      cmm<false, true>(br, bi, pr[1], pi[1], e1t);
      const float sc = real ? 0.5f : 1.f;
      const unsigned zp = WS_Z + (it == 2 ? 512 : c * 16 + it * 8 + wv) * 8192 + lane * 16;
#pragma unroll
      for (int g = 0; g < 4; ++g) {
        u32x4 o0, o1;
#pragma unroll
        for (int n = 0; n < 4; ++n) { o0[n] = pack2(ar[4 * g + n] * sc, ai[4 * g + n]); o1[n] = pack2(br[4 * g + n] * sc, bi[4 * g + n]); }
        st_sc1(rs, zp + g * 2048, o0); st_sc1(rs, zp + g * 2048 + 1024, o1);
      }
      if (it == 0) { WAIT_L0; BAR; }
      FENCE_C;
    }
  }
  STAMP(4);
  WAIT_V(0); BAR;
  if (threadIdx.x == 0) __hip_atomic_store((gu64*)(ws + WS_FZ) + c, ep, RLX_AGENT);
  {
    const int lane = otid() & 63, r = lane & 31, h = lane >> 5;
    const int g = c >> 3, bz = (c >> 2) & 1, hz = (c >> 1) & 1;
    bf16x8 b1[2], b2[2], ec[2], ens[2]; float twc[16], tws[16];
#pragma unroll
    for (int ks = 0; ks < 2; ++ks) { ec[ks] = LDT(tb, ks); ens[ks] = neg8(LDT(tb, 2 + ks)); b1[ks] = LDT(tb, 20 + ks); b2[ks] = LDT(tb, 22 + ks); }
    LDTW(twc, tb, 24); LDTW(tws, tb, 28);
    if (wv == 0) poll_flags<0>(ws, WS_FZ, ep);
    BAR; FENCE_C;
    STAMP(5);
    u32x4 nyq[2];
    {
      u32x4 t[16];
#pragma unroll
      for (int i = 0; i < 16; ++i) t[i] = ld_sc1(rs, WS_Z + ((wv * 16 + i) * 4 + (lane >> 4)) * 8192 + c * 256 + (lane & 15) * 16);
#pragma unroll
      for (int o = 0; o < 2; ++o) nyq[o] = ld_sc1(rs, WS_Z + 512 * 8192 + c * 256 + (wv + 8 * o) * 16);
#pragma unroll
      for (int i = 0; i < 16; ++i) { const int k2 = (wv * 16 + i) * 4 + (lane >> 4); *(LAS u32x4*)(smem + k2 * 256 + (((lane & 15) ^ (k2 & 15)) * 16)) = t[i]; }
    }
    WAIT_L0; BAR; SCHED;
    STAMP(6);
#pragma unroll
    for (int o = 0; o < 2; ++o) {
      const int ro = wv + 8 * o;
      u32x4 z[2][4];
#pragma unroll
      for (int ks = 0; ks < 2; ++ks)
#pragma unroll
        for (int u = 0; u < 4; ++u) z[ks][u] = *(LAS const u32x4*)(smem + (r + 32 * (8 * h + 4 * ks + u)) * 256 + ((ro ^ (r & 15)) * 16));
#pragma unroll
      for (int n = 0; n < 4; ++n) {
        f32x16 ur = zero16(), ui = zero16();
#pragma unroll
        for (int ks = 0; ks < 2; ++ks) { const bf16x8 za = mk8(z[ks][0][n], z[ks][1][n], z[ks][2][n], z[ks][3][n]); ur = MFMA32(za, b1[ks], ur); ui = MFMA32(za, b2[ks], ui); }
        const float ny = bf_lo(nyq[o][n]);
        f32x16 vr, vi;
#pragma unroll
        for (int i = 0; i < 16; ++i) { vr[i] = ur[i] * twc[i] - ui[i] * tws[i]; vi[i] = ui[i] * twc[i] + ur[i] * tws[i]; }
        f32x16 y;
#pragma unroll
        for (int i = 0; i < 16; ++i) y[i] = (i & 1) ? -ny : ny;
#pragma unroll
        for (int ks = 0; ks < 2; ++ks) { y = MFMA32(pack_step(vr, ks), ec[ks], y); y = MFMA32(pack_step(vi, ks), ens[ks], y); }
        const int tok = 64 * (8 * g + 4 * hz + n) + 32 * bz + (c & 1) * 16 + ro;
        u16* po = (u16*)(ws + WS_Y) + (size_t)tok * D_ + r * 32 + h * 16;
        *(bf16x8*)po = pack_step(y, 0); *(bf16x8*)(po + 8) = pack_step(y, 1);
      }
    }
  }
}

#define HAUX 16
#define DOT4(acc, v, tagstr) do { const u32x4 u_ = __builtin_bit_cast(u32x4, v); \
    asm volatile("v_dot2c_f32_bf16 %0, %1, %2\n\tv_dot2c_f32_bf16 %0, %1, %3\n\tv_dot2c_f32_bf16 %0, %1, %4\n\tv_dot2c_f32_bf16 %0, %1, %5\n\ts_nop 2 ; " tagstr \
                 : "+v"(acc) : "s"(0x3f803f80u), "v"(u_[0]), "v"(u_[1]), "v"(u_[2]), "v"(u_[3])); } while (0)
DI void gemm1_group(LAS unsigned char* lds, const u16* __restrict__ x, const u16* __restrict__ w_r, const u16* __restrict__ w_i, const u16* __restrict__ wg, const u16* __restrict__ wu,
                    const u16* __restrict__ wd, unsigned char* __restrict__ ws, __amdgpu_buffer_rsrc_t rs STAMP_ARG) {
  const int wid = __builtin_amdgcn_readfirstlane(threadIdx.x >> 6), wr = wid >> 2, wc = wid & 3;
  const int w = blockIdx.x, xcd = w & 7, idx = w >> 3;
  const int pm = (xcd & 1) * 4 + (idx & 3), pn = (xcd >> 1) * 7 + (idx >> 2);
  const int rot = (pm >> 2) * 8, tcs = (pm & 3) * 2;
  unsigned voffA[2], voffB[2];
  const unsigned ldsw = (unsigned)(size_t)lds + (unsigned)wid * 1024u;
  const __amdgpu_buffer_rsrc_t rA = MKRSRC(x, S_ * D_ * 2), rG = MKRSRC(wg, D_ * F_ * 2), rU = MKRSRC(wu, D_ * F_ * 2), rD = MKRSRC(wd, F_ * D_ * 2);
  const unsigned cA = pm * 256 * D_ * 2, cB = pn * 128 * 2;
  constexpr unsigned hA = 128 * D_ * 2, kA = 128, kB = 64 * F_ * 2;
#define SA(b, h) (((b) * 2 + (h)) * HTB)
#define SB(b, h) ((4 + (b) * 2 + (h)) * HTB)
#define STAGE(bufoff, rsrc, soff, voff) do { DMA16P(rsrc, (voff)[0], soff, ldsw, (bufoff), POL_##rsrc); DMA16P(rsrc, (voff)[1], soff, ldsw, (bufoff) + 8192, POL_##rsrc); } while (0)
  f32x4 acc[2][2][4][2];
  int aoff0, aoff1, boff0, boff1;
  const unsigned a0 = cA + rot * kA, b0 = cB + rot * kB;
  const unsigned long long ep = row_fwd<4, 4, 2>([&](int q) {
    if (q == 0) {
      const int tid = otid(), lane = tid & 63, fr = lane & 15, fq = lane >> 4;
#pragma unroll
      for (int i = 0; i < 2; ++i) {
        const int b = tid * 16 + i * 8192;
        const int ra = b >> 7, ca = ((b >> 4) & 7) ^ ((ra >> 1) & 7);
        voffA[i] = (unsigned)(ra * D_ + ca * 8) * 2u;
        const int kb = b >> 8, p = (b >> 4) & 15, u = (p >> 1) ^ ((kb & 3) | (((kb >> 3) & 1) << 2));
        voffB[i] = (unsigned)(kb * F_ + u * 16 + (p & 1) * 8) * 2u;
      }
      STAGE(SB(0, 0), rG, b0, voffB); STAGE(SA(0, 0), rA, a0, voffA);
      const int q4 = (lane & 15) >> 2, pp = lane & 3;
      aoff0 = (wr * 64 + fr) * 128 + ((fq ^ (fr >> 1)) * 16); aoff1 = aoff0 ^ 64;
      boff0 = (8 * fq + q4) * 256 + (((wc * 2) ^ (q4 | ((fq & 1) << 2))) * 32) + pp * 8; boff1 = boff0 ^ 32;
      PIN(aoff0); PIN(aoff1); PIN(boff0); PIN(boff1);
    } else if (q == 1) { STAGE(SB(0, 1), rU, b0, voffB); STAGE(SA(0, 1), rA, a0 + hA, voffA); }
    else STAGE(SB(1, 0), rG, b0 + kB, voffB);
    if (q != 1) {
#pragma unroll
      for (int i = q ? 24 : 0; i < (q ? 32 : 24); ++i) { acc[i >> 4][(i >> 3) & 1][(i >> 1) & 3][i & 1] = (f32x4){0.f, 0.f, 0.f, 0.f}; PIN(acc[i >> 4][(i >> 3) & 1][(i >> 1) & 3][i & 1]); }
    }
  }, lds, K1_SCR, x, w_r, w_i, ws, rs STAMP_PASS);
  STAGE(SA(1, 0), rA, a0 + kA, voffA); STAGE(SB(1, 1), rU, b0 + kB, voffB);
  const unsigned tag = (unsigned)(ep >> 32);
  STAMP(1);
#define LDA(dst, b, h) do { _Pragma("unroll") for (int m = 0; m < 4; ++m) { dst[m][0] = *(const LAS bf16x8*)(lds + SA(b, h) + aoff0 + m * 2048); dst[m][1] = *(const LAS bf16x8*)(lds + SA(b, h) + aoff1 + m * 2048); } } while (0)
#define LDB(dst, b, h) do { _Pragma("unroll") for (int k = 0; k < 2; ++k) { \
    dst[0][k] = cat4(tr_read(lds + SB(b, h) + boff0 + k * 8192), tr_read(lds + SB(b, h) + boff0 + k * 8192 + 1024)); \
    dst[1][k] = cat4(tr_read(lds + SB(b, h) + boff1 + k * 8192), tr_read(lds + SB(b, h) + boff1 + k * 8192 + 1024)); } } while (0)
#define MMA(ai, bj, At, Bt) do { __builtin_amdgcn_s_setprio(1); _Pragma("unroll") for (int m = 0; m < 4; ++m) _Pragma("unroll") for (int n = 0; n < 2; ++n) _Pragma("unroll") for (int k = 0; k < 2; ++k) \
    acc[ai][bj][m][n] = MFMA16(Bt[n][k], At[m][k], acc[ai][bj][m][n]); __builtin_amdgcn_s_setprio(0); } while (0)
#define CSUM do { if (wr == 0) { DOT4(cs0, B0[0][0], "g"); DOT4(cs1, B0[1][0], "g"); DOT4(cs0, B0[0][1], "g"); DOT4(cs1, B0[1][1], "g"); } \
    else { DOT4(cs0, B1[0][0], "u"); DOT4(cs1, B1[1][0], "u"); DOT4(cs0, B1[0][1], "u"); DOT4(cs1, B1[1][1], "u"); } } while (0)
  bf16x8 At[4][2], B0[2][2], B1[2][2];
  constexpr int nt = D_ / 64;
  WAIT_V(12); BAR;
  if (wr == 1) BAR;
  STAMP(2); STAMPB(4);
  for (int t = 0; t < nt - 2; t += 2) {
    const unsigned k2 = (t + 2 + rot) & 15;
    const unsigned a1 = cA + (((t + rot) & 15) + 1) * kA, a2 = cA + k2 * kA, a3 = a2 + kA, b2 = cB + k2 * kB, b3 = b2 + kB;
    float cs0 = 0.f, cs1 = 0.f;
    LDB(B0, 0, 0); SCHED; LDA(At, 0, 0); STAGE(SA(1, 1), rA, a1 + hA, voffA);
    WAIT_L(8); BAR; WAIT_L(0); MMA(0, 0, At, B0); BAR; SCHED;
    LDB(B1, 0, 1); STAGE(SB(0, 0), rG, b2, voffB);
    BAR; WAIT_L(0); MMA(0, 1, At, B1); BAR;
    LDA(At, 0, 1); STAGE(SA(0, 0), rA, a2, voffA);
    BAR; WAIT_L(0); MMA(1, 0, At, B0); BAR; SCHED;
    STAGE(SB(0, 1), rU, b2, voffB);
    if (t == tcs) CSUM;
    WAIT_V(6); BAR; MMA(1, 1, At, B1); BAR;
    if (t == 0 && threadIdx.x == 0) __hip_atomic_store((gu64*)(ws + WS_FA) + w, ep, RLX_AGENT);
    if (t == 0) STAMPB(5);
    if (t == 2) STAMPB(6);
    if (t >= 6) DMA16(rD, otid() * 16, w * 32768 + (t - 6) * 4096, ldsw, K1_SCR + 16384);
    LDB(B0, 1, 0); SCHED; LDA(At, 1, 0); STAGE(SA(0, 1), rA, a2 + hA, voffA);
    WAIT_L(8); BAR; WAIT_L(0); MMA(0, 0, At, B0); BAR; SCHED;
    LDB(B1, 1, 1); STAGE(SB(1, 0), rG, b3, voffB);
    BAR; WAIT_L(0); MMA(0, 1, At, B1); BAR;
    LDA(At, 1, 1); STAGE(SA(1, 0), rA, a3, voffA);
    BAR; WAIT_L(0); MMA(1, 0, At, B0); BAR; SCHED;
    STAGE(SB(1, 1), rU, b3, voffB);
    WAIT_V(6);
    if (t == tcs) {
      CSUM;
      const u32x2 p = __builtin_amdgcn_permlane16_swap(__float_as_uint(cs0), __float_as_uint(cs1), false, false);
      const unsigned e = __float_as_uint(__uint_as_float(p[0]) + __uint_as_float(p[1]));
      const u32x2 q = __builtin_amdgcn_permlane32_swap(e, e, false, false);
      const unsigned c = __float_as_uint(__uint_as_float(q[0]) + __uint_as_float(q[1]));
      const int lane = otid() & 63;
      if (lane < 32) __hip_atomic_store((gu64*)(ws + WS_CS) + (pn * 8 + pm) * 256 + wid * 32 + lane, ((unsigned long long)tag << 32) | c, RLX_AGENT);
    }
    BAR; MMA(1, 1, At, B1); BAR;
  }
  STAMP(3);
  u32x2 gr[5];
#define LDGR do { const unsigned go_ = (otid() & 255) * 8; _Pragma("unroll") for (int i = 0; i < 4; ++i) gr[i] = __builtin_amdgcn_raw_buffer_load_b64(rs, go_ + i * 2048, WS_CS + (pn * 8 + wr * 4) * 2048, 16); \
    gr[4] = __builtin_amdgcn_raw_buffer_load_b64(rs, go_, WS_STAT + pm * 2048, 16); } while (0)
  { LDB(B0, 0, 0); LDA(At, 0, 0); STAGE(SA(1, 1), rA, cA + (((nt - 2 + rot) & 15) + 1) * kA + hA, voffA);
    LDGR;
    BAR; WAIT_L(0); MMA(0, 0, At, B0); BAR;
    LDB(B1, 0, 1); BAR; WAIT_L(0); MMA(0, 1, At, B1); BAR;
    LDA(At, 0, 1); WAIT_V(7); BAR; WAIT_L(0); MMA(1, 0, At, B0); MMA(1, 1, At, B1); BAR; }
  { LDB(B0, 1, 0); LDA(At, 1, 0); BAR; WAIT_L(0); MMA(0, 0, At, B0); BAR;
    LDB(B1, 1, 1); WAIT_V(0); BAR; WAIT_L(0); MMA(0, 1, At, B1); BAR;
    LDA(At, 1, 1);
#pragma unroll 1
    for (unsigned spins = 0; spins < SPIN_CAP; ++spins) {
      bool ok = true;
#pragma unroll
      for (int i = 0; i < 5; ++i) ok &= gr[i][1] == tag;
      if (__all(ok)) break;
      __builtin_amdgcn_s_sleep(2); FENCE_C;
      LDGR;
    }
    const int tid = otid();
    *(LAS float*)(lds + K1_SCR + tid * 4) = (__uint_as_float(gr[0][0]) + __uint_as_float(gr[1][0])) + (__uint_as_float(gr[2][0]) + __uint_as_float(gr[3][0]));
    if (wr == 0) {
      const float den = bf_hi(gr[4][0]), rd = __builtin_amdgcn_rcpf(den), k1 = -2.f * 0.7978845608028654f * 1.4426950408889634f;
      *(LAS f32x4*)(lds + K1_SCR + 2048 + tid * 16) = (f32x4){-bf_lo(gr[4][0]), k1 * rd, k1 * 0.044715f * rd * rd * rd, den * den};
    }
    WAIT_L(0); BAR; MMA(1, 0, At, B0); MMA(1, 1, At, B1); BAR; }
  if (wr == 0) BAR;
  FENCE_C;
  STAMP(4);
  const int lane = otid() & 63, fr = lane & 15, fq = lane >> 4;
  f32x4 cg[2], cu[2];
#pragma unroll
  for (int n = 0; n < 2; ++n) {
    LAS const unsigned char* p = lds + K1_SCR + (wc * 32 + n * 16 + fq * 4) * 4;
    cg[n] = *(LAS const f32x4*)p + *(LAS const f32x4*)(p + 1024); cu[n] = *(LAS const f32x4*)(p + 512) + *(LAS const f32x4*)(p + 1536);
  }
#pragma unroll
  for (int ai = 0; ai < 2; ++ai)
#pragma unroll
    for (int m = 0; m < 4; ++m) {
      const f32x4 sv = *(LAS const f32x4*)(lds + K1_SCR + 2048 + (ai * 128 + wr * 64 + m * 16 + fr) * 16);
      u32x4 o;
#pragma unroll
      for (int n = 0; n < 2; ++n) {
        float hv[4];
#pragma unroll
        for (int i = 0; i < 4; ++i) {
          const float gs = acc[ai][0][m][n][i] + sv[0] * cg[n][i], us = acc[ai][1][m][n][i] + sv[0] * cu[n][i];
          const float e = __builtin_amdgcn_exp2f(gs * (sv[1] + sv[2] * (gs * gs)));
          hv[i] = gs * us * __builtin_amdgcn_rcpf(sv[3] + sv[3] * e);
        }
        o[2 * n] = pack2(hv[0], hv[1]); o[2 * n + 1] = pack2(hv[2], hv[3]);
      }
      __builtin_amdgcn_raw_buffer_store_b128(o, rs, WS_H + (((pm * 2 + ai) * 112 + pn * 4 + wc) * 8 + wr * 4 + m) * 1024 + lane * 16, 0, HAUX);
    }
  STAMP(5);
#undef SA
#undef SB
#undef STAGE
#undef LDA
#undef LDB
#undef MMA
}
__global__ __launch_bounds__(512, 2) void k1_main(const u16* __restrict__ x, const u16* __restrict__ w_r, const u16* __restrict__ w_i, const u16* __restrict__ wg,
                                                  const u16* __restrict__ wu, const u16* __restrict__ wd, unsigned char* __restrict__ ws) {
  extern __shared__ __attribute__((aligned(16))) unsigned char smem_[];
  LAS unsigned char* lds = (LAS unsigned char*)smem_;
  STAMP_DECL; STAMP(0);
  asm volatile("" :: "s"(x), "s"(w_r), "s"(w_i), "s"(wg), "s"(wu), "s"(wd), "s"(ws));
  const __amdgpu_buffer_rsrc_t rs = __builtin_amdgcn_make_buffer_rsrc((void*)ws, 0, WS_USED, 0x00020000);
  if (blockIdx.x < 224) {
    gemm1_group(lds, x, w_r, w_i, wg, wu, wd, ws, rs STAMP_PASS);
    STAMP_FLUSH(0);
  } else {
    fft_group(lds, x, w_r, w_i, ws, rs STAMP_PASS);
    STAMP_FLUSH(1);
  }
}

constexpr int G2_SLOT = 24576, G2_NS = 6, G2_NT = F_ / 64;
__global__ __launch_bounds__(512, 2) void k2_gemm2(const u16* __restrict__ x, const u16* __restrict__ wd, const unsigned char* __restrict__ ws, u16* __restrict__ out) {
  extern __shared__ __attribute__((aligned(16))) unsigned char smem_[];
  LAS unsigned char* lds = (LAS unsigned char*)smem_;
  const int tid = threadIdx.x, wid = __builtin_amdgcn_readfirstlane(tid >> 6), lane = tid & 63;
  const int xcd = blockIdx.x & 7, idx = blockIdx.x >> 3;
  const int tm = (xcd >> 1) * 4 + (idx & 3), tn = (xcd & 1) * 8 + (idx >> 2);
  STAMP_DECL; STAMP(0);
  const int tok = tid >> 2, dq = tid & 3;
  const size_t go = (size_t)(tm * 128 + tok) * D_ + tn * 64 + dq * 16;
  u32x4 b0, b1, x0, x1;
#define LDRES do { b0 = *(const u32x4*)((const u16*)(ws + WS_Y) + go); b1 = *(const u32x4*)((const u16*)(ws + WS_Y) + go + 8); x0 = *(const u32x4*)(x + go); x1 = *(const u32x4*)(x + go + 8); } while (0)
  if (wid >= 4) {
    const int lw = wid - 4, lt = tid - 256;
    const __amdgpu_buffer_rsrc_t rH = MKRSRC(ws + WS_H, S_ * F_ * 2), rD = MKRSRC(wd, F_ * D_ * 2);
    const int wrow = lt >> 3, wp = lt & 7;
    const unsigned hv = lt * 16, wvo = (wrow * D_ + (((wp >> 1) ^ ((wrow >> 1) & 3)) * 16) + (wp & 1) * 8) * 2;
    const unsigned hs = tm * 112 * 8192, wso = tn * 128;
    const unsigned ldsw = (unsigned)(size_t)lds + lw * 1024u;
#define STAGE2(t, slot) do { DMA16(rD, wvo, wso + (t) * (64 * D_ * 2), ldsw, (slot) * G2_SLOT + 16384); DMA16(rD, wvo, wso + (t) * (64 * D_ * 2) + 32 * D_ * 2, ldsw, (slot) * G2_SLOT + 16384 + 4096); \
      DMA16(rH, hv, hs + (t) * 16384, ldsw, (slot) * G2_SLOT); DMA16(rH, hv, hs + (t) * 16384 + 4096, ldsw, (slot) * G2_SLOT + 4096); \
      DMA16(rH, hv, hs + (t) * 16384 + 8192, ldsw, (slot) * G2_SLOT + 8192); DMA16(rH, hv, hs + (t) * 16384 + 12288, ldsw, (slot) * G2_SLOT + 12288); } while (0)
    STAGE2(0, 0); STAGE2(1, 1); STAGE2(2, 2); STAGE2(3, 3); STAGE2(4, 4); STAGE2(5, 5);
    WAIT_V(30); BAR;
    WAIT_V(24); BAR;
    for (int t = 0; t < 48; t += 6) {
      STAGE2(t + 6, 0); WAIT_V(24); BAR;
      STAGE2(t + 7, 1); WAIT_V(24); BAR;
      STAGE2(t + 8, 2); WAIT_V(24); BAR;
      STAGE2(t + 9, 3); WAIT_V(24); BAR;
      STAGE2(t + 10, 4); WAIT_V(24); BAR;
      STAGE2(t + 11, 5); WAIT_V(24); BAR;
    }
    STAGE2(54, 0); WAIT_V(24); BAR;
    STAGE2(55, 1); WAIT_V(24); BAR;
    WAIT_V(18); BAR;
    WAIT_V(12); BAR;
    WAIT_V(6); BAR;
    WAIT_V(0); BAR;
    LDRES;
    BAR;
  } else {
    const int wm = wid >> 1, wn = wid & 1, fq = lane >> 4, fr = lane & 15, q4 = (lane & 15) >> 2, pp = lane & 3;
    const int hoff = wm * 4096 + lane * 16;
    const int woff0 = 16384 + (4 * fq + q4) * 128 + (((wn * 2) ^ ((2 * fq + (q4 >> 1)) & 3)) * 32) + pp * 8, woff1 = woff0 ^ 32;
#define LDH(H, slot, k) do { _Pragma("unroll") for (int m = 0; m < 4; ++m) H[k][m] = *(const LAS bf16x8*)(lds + (slot) * G2_SLOT + (k) * 8192 + hoff + m * 1024); } while (0)
#define LDW(W, slot, k) do { W[k][0] = cat4(tr_read(lds + (slot) * G2_SLOT + (k) * 4096 + woff0), tr_read(lds + (slot) * G2_SLOT + (k) * 4096 + woff0 + 2048)); \
      W[k][1] = cat4(tr_read(lds + (slot) * G2_SLOT + (k) * 4096 + woff1), tr_read(lds + (slot) * G2_SLOT + (k) * 4096 + woff1 + 2048)); } while (0)
#define MM2(H, W, k) do { _Pragma("unroll") for (int m = 0; m < 4; ++m) _Pragma("unroll") for (int n = 0; n < 2; ++n) acc[m][n] = MFMA16(W[k][n], H[k][m], acc[m][n]); } while (0)
    f32x4 acc[4][2];
#pragma unroll
    for (int m = 0; m < 4; ++m) { acc[m][0] = (f32x4){0.f, 0.f, 0.f, 0.f}; acc[m][1] = (f32x4){0.f, 0.f, 0.f, 0.f}; }
    bf16x8 Ha[2][4], Wa[2][2], Hb[2][4], Wb[2][2];
    BAR;
    LDW(Wa, 0, 0); LDH(Ha, 0, 0); LDW(Wa, 0, 1); LDH(Ha, 0, 1);
    WAIT_L0; BAR; SCHED;
    STAMP(1);
#define ITER(nslot, Hc, Wc, Hn, Wn) do { LDW(Wn, nslot, 0); LDH(Hn, nslot, 0); SCHED; MM2(Hc, Wc, 0); SCHED; LDW(Wn, nslot, 1); LDH(Hn, nslot, 1); SCHED; MM2(Hc, Wc, 1); SCHED; WAIT_L0; BAR; SCHED; } while (0)
    for (int t = 0; t < 54; t += 6) {
      ITER(1, Ha, Wa, Hb, Wb);
      ITER(2, Hb, Wb, Ha, Wa);
      ITER(3, Ha, Wa, Hb, Wb);
      ITER(4, Hb, Wb, Ha, Wa);
      ITER(5, Ha, Wa, Hb, Wb);
      ITER(0, Hb, Wb, Ha, Wa);
    }
    STAMP(2);
    LDRES;
    ITER(1, Ha, Wa, Hb, Wb);
    MM2(Hb, Wb, 0); MM2(Hb, Wb, 1);
    STAMP(3);
#pragma unroll
    for (int m = 0; m < 4; ++m)
#pragma unroll
      for (int n = 0; n < 2; ++n) *(LAS f32x4*)(lds + (wm * 64 + m * 16 + fr) * 272 + (wn * 32 + n * 16 + fq * 4) * 4) = acc[m][n];
  }
  __syncthreads();
  u32x4 o[2];
#pragma unroll
  for (int i = 0; i < 4; ++i) {
    const f32x4 a = *(const LAS f32x4*)(lds + tok * 272 + (dq * 16 + i * 4) * 4);
    const unsigned p0 = pack2(a[0], a[1]), p1 = pack2(a[2], a[3]);
    const unsigned y0 = (i < 2 ? b0 : b1)[(2 * i) & 3], y1 = (i < 2 ? b0 : b1)[(2 * i + 1) & 3], r0 = (i < 2 ? x0 : x1)[(2 * i) & 3], r1 = (i < 2 ? x0 : x1)[(2 * i + 1) & 3];
    const unsigned c0 = pack2(bf_lo(r0) + bf_lo(y0), bf_hi(r0) + bf_hi(y0)), c1 = pack2(bf_lo(r1) + bf_lo(y1), bf_hi(r1) + bf_hi(y1));
    o[i >> 1][(2 * i) & 3] = pack2(bf_lo(c0) + bf_lo(p0), bf_hi(c0) + bf_hi(p0));
    o[i >> 1][(2 * i + 1) & 3] = pack2(bf_lo(c1) + bf_lo(p1), bf_hi(c1) + bf_hi(p1));
  }
  *(u32x4*)(out + go) = o[0]; *(u32x4*)(out + go + 8) = o[1];
  STAMP(4);
  STAMP_FLUSH(4);
}

extern "C" void kernel_launch(void* const* d_in, const int* in_sizes, int n_in, void* d_out, int out_size, void* d_ws, size_t ws_size, hipStream_t stream) {
  const u16* x = (const u16*)d_in[0]; const u16* w_r = (const u16*)d_in[1]; const u16* w_i = (const u16*)d_in[2];
  const u16* wg = (const u16*)d_in[4]; const u16* wu = (const u16*)d_in[5]; const u16* wd = (const u16*)d_in[6];
  unsigned char* ws = (unsigned char*)d_ws;
  (void)hipFuncSetAttribute((const void*)k1_main, hipFuncAttributeMaxDynamicSharedMemorySize, K1_LDS);
  (void)hipFuncSetAttribute((const void*)k2_gemm2, hipFuncAttributeMaxDynamicSharedMemorySize, G2_SLOT * G2_NS);
  k1_main<<<256, 512, K1_LDS, stream>>>(x, w_r, w_i, wg, wu, wd, ws);
  k2_gemm2<<<256, 512, G2_SLOT * G2_NS, stream>>>(x, wd, ws, (u16*)d_out);
}
```

```cpp
#include <hip/hip_runtime.h>
#include <hip/hip_bf16.h>
#include <stdint.h>

#define LAS __attribute__((address_space(3)))
#define DI __device__ __forceinline__
typedef unsigned short u16;
typedef __bf16 bf16x8 __attribute__((ext_vector_type(8)));
typedef __bf16 bf16x4 __attribute__((ext_vector_type(4)));
typedef __bf16 bf16x2 __attribute__((ext_vector_type(2)));
typedef float f32x2 __attribute__((ext_vector_type(2)));
typedef float f32x4 __attribute__((ext_vector_type(4)));
typedef float f32x16 __attribute__((ext_vector_type(16)));
typedef unsigned u32x2 __attribute__((ext_vector_type(2)));
typedef unsigned u32x4 __attribute__((ext_vector_type(4)));
typedef unsigned u32x4a __attribute__((ext_vector_type(4), aligned(4)));

constexpr int S_ = 2048, D_ = 1024, F_ = 3584, DF_ = 513;
constexpr unsigned WS_CTL = 0;
#define DUP 1
constexpr unsigned WS_FA = 4096;
constexpr unsigned WS_FZ = 6144;
constexpr unsigned WS_FY = 6400;
constexpr unsigned WS_FH = 49152;
#define FLAG(off) ((gu64*)(ws + (off)))
#define SETFLAG(l, off, n) do { const unsigned l_ = (l); if (l_ < (n)) __hip_atomic_store(FLAG(off) + l_ * 1024, ep, RLX_AGENT); } while (0)
constexpr unsigned WS_STAT = 16384;
constexpr unsigned WS_NYQ = 1u << 20;
constexpr unsigned WS_CS = 65536;
constexpr unsigned WS_H = 4u << 20;
constexpr unsigned WS_RB = 20u << 20;
constexpr unsigned WS_Z = 28u << 20;
constexpr unsigned WS_Y = 33u << 20;
constexpr unsigned WS_P = 38u << 20;
constexpr unsigned WS_USED = 46u << 20;
typedef __attribute__((address_space(1))) unsigned long long gu64;
typedef __attribute__((address_space(1))) unsigned gu32;
#define RLX_AGENT __ATOMIC_RELAXED, __HIP_MEMORY_SCOPE_AGENT
constexpr unsigned SPIN_CAP = 1u << 16;
constexpr int HTB = 16384, K1_SCR = 131072, K1_LDS = 163840;

DI unsigned pack2(float a, float b) { f32x2 v = {a, b}; return __builtin_bit_cast(unsigned, __builtin_convertvector(v, bf16x2)); }
DI float bf_lo(unsigned u) { return __uint_as_float(u << 16); }
DI float bf_hi(unsigned u) { return __uint_as_float(u & 0xffff0000u); }
DI float rbf(float a) { return bf_lo(pack2(a, a)); }
constexpr int crow(int reg, int h) { return (reg & 3) + 8 * (reg >> 2) + 4 * h; }
constexpr int krow(int s, int h, int j) { return 16 * s + 8 * (j >> 2) + 4 * h + (j & 3); }
struct Trig {
  float f[2048]; unsigned short b[2048];
  constexpr Trig() : f(), b() {
    for (int k = 0; k <= 512; ++k) {
      const bool sw = k > 256;
      const double x = (sw ? 512 - k : k) * (6.283185307179586476925 / 2048), x2 = x * x;
      double t = sw ? x : 1.0, s = t;
      for (int n = sw ? 2 : 1; n < 22; n += 2) { t *= -x2 / (n * (n + 1)); s += t; }
      const float v = (float)s;
      f[k] = v; f[(2048 - k) & 2047] = v; f[1024 - k] = -v; f[1024 + k] = -v;
    }
    f[512] = 0.f; f[1536] = 0.f;
    for (int k = 0; k < 2048; ++k) { const unsigned u = __builtin_bit_cast(unsigned, f[k]); b[k] = (unsigned short)((u + 0x7fffu + ((u >> 16) & 1)) >> 16); }
  }
};
constexpr Trig TG;
struct Tabs {
  unsigned v[64 * 256];
  static constexpr int ang(int m, int ks, int r, int h, int e) {
    const int a = krow(ks, h, e), b = 16 * ((r >> 2) & 1) + (r & 3) + 4 * (r >> 3), kb = 8 * h + 4 * ks + (e >> 1);
    const int c = 8 * (ks & 3) + 4 * h + (e >> 1), ph = 512 * (ks >> 2) - 512 * (e & 1);
    switch (m) {
      case 0: return 64 * a * r;
      case 1: return 64 * a * r + 512;
      case 2: return 64 * a * (r >> 1) + 512 * (r & 1);
      case 3: return 64 * a * (r >> 1) + 512 * (r & 1) - 512;
      case 4: return 64 * c * r + ph;
      case 5: return 32 * (2 * c + 1) * r + ph;
      case 6: return 32 * (2 * r + 1) * c + ph;
      case 9: return 64 * kb * b + 512 * (e & 1);
      default: return 64 * kb * b + 512 * (e & 1) - 512;
    }
  }
  constexpr void frag(int q, int m, int n = 2) {
    for (int ks = 0; ks < n; ++ks)
      for (int l = 0; l < 64; ++l)
        for (int d = 0; d < 4; ++d)
          v[(q + ks) * 256 + l * 4 + d] = TG.b[ang(m, ks, l & 31, l >> 5, 2 * d) & 2047] | (unsigned)TG.b[ang(m, ks, l & 31, l >> 5, 2 * d + 1) & 2047] << 16;
  }
  constexpr void tw(int q, int m) {
    for (int l = 0; l < 64; ++l)
      for (int i = 0; i < 16; ++i) {
        const int r = l & 31, a = crow(i, l >> 5) * (m == 0 ? 2 * r : m == 1 ? r : 2 * (16 * ((r >> 2) & 1) + (r & 3) + 4 * (r >> 3)));
        const float sc = m == 1 ? 1.f / 1024.f : 1.f;
        v[(q + (i >> 2)) * 256 + l * 4 + (i & 3)] = __builtin_bit_cast(unsigned, TG.f[a & 2047] * sc);
        v[(q + 4 + (i >> 2)) * 256 + l * 4 + (i & 3)] = __builtin_bit_cast(unsigned, TG.f[(a - 512) & 2047] * sc);
      }
  }
  constexpr Tabs() : v() {
    frag(0, 0); frag(2, 1); frag(4, 2); frag(6, 3); tw(8, 0);
    tw(16, 1); frag(24, 5, 8); frag(32, 6, 8); frag(40, 4, 8);
    frag(48, 0); frag(50, 1); frag(52, 9); frag(54, 10); tw(56, 2);
  }
};
static __device__ const __attribute__((aligned(128))) Tabs g_tab;
DI bf16x8 mk8(unsigned a, unsigned b, unsigned c, unsigned d) { u32x4 v = {a, b, c, d}; return __builtin_bit_cast(bf16x8, v); }
DI bf16x8 pack_step(const f32x16& x, int s) { return mk8(pack2(x[8 * s], x[8 * s + 1]), pack2(x[8 * s + 2], x[8 * s + 3]), pack2(x[8 * s + 4], x[8 * s + 5]), pack2(x[8 * s + 6], x[8 * s + 7])); }
DI bf16x4 tr_read(LAS const unsigned char* p) { return __builtin_amdgcn_ds_read_tr16_b64_v4bf16((LAS bf16x4*)p); }
DI bf16x8 cat4(bf16x4 a, bf16x4 b) { return __builtin_shufflevector(a, b, 0, 1, 2, 3, 4, 5, 6, 7); }
#define MFMA32(a, b, c) __builtin_amdgcn_mfma_f32_32x32x16_bf16((a), (b), (c), 0, 0, 0)
#define MFMA16(a, b, c) __builtin_amdgcn_mfma_f32_16x16x32_bf16((a), (b), (c), 0, 0, 0)
#define GLDS16(g, l) __builtin_amdgcn_global_load_lds((const unsigned*)(g), (LAS unsigned*)(l), 16, 0, 0)
#define DMA16P(rsrc, voff, soff, lbase, limm, pol) asm volatile("s_add_i32 m0, %0, %4\n\ts_nop 0\n\tbuffer_load_dwordx4 %1, %2, %3 offen" pol " lds" :: "s"(lbase), "v"(voff), "s"(rsrc), "s"(soff), "i"(limm) : "scc", "memory")
#define DMA16(rsrc, voff, soff, lbase, limm) DMA16P(rsrc, voff, soff, lbase, limm, "")
#define POLX " nt"
#define POLW " nt"
#define POLA ""
#define POLB " nt"
#define POLPF ""
#define XP 0
#define PFW 1
#define WLD(p) (*(p))
#define PMX 0
#define PMG(x) (((x) & 1) ^ PMX)
#define POL_rA POLA
#define POL_rG POLB
#define POL_rU POLB
#define MKRSRC(p, n) __builtin_amdgcn_make_buffer_rsrc((void*)(p), 0, (n), 0x00020000)
#define FENCE_C asm volatile("" ::: "memory")
#define WAIT_V(n) asm volatile("s_waitcnt vmcnt(" #n ")" ::: "memory")
#define WAIT_L(n) asm volatile("s_waitcnt lgkmcnt(" #n ")" ::: "memory")
#define WAIT_L0 __builtin_amdgcn_s_waitcnt(0xC07F)
#define BAR __builtin_amdgcn_s_barrier()
#define SCHED __builtin_amdgcn_sched_barrier(0)

#define STAMP_DECL
#define STAMP_ARG
#define STAMP_PASS
#define STAMP(s)
#define STAMPB(s)
#define STAMPT(s)
#define STAMP_FLUSH(k)
#define STAMP_RESET
#define PIN(v) asm volatile("" : "+v"(v))
#define PIN16(a) do { _Pragma("unroll") for (int i_ = 0; i_ < 16; ++i_) PIN(a[i_]); } while (0)
DI int otid() { int t = threadIdx.x; PIN(t); return t; }
DI f32x16 zero16() { f32x16 z; for (int i = 0; i < 16; ++i) z[i] = 0.f; return z; }
template <int CTRL, int RM> DI float dpp_add(float v) { return v + __builtin_bit_cast(float, __builtin_amdgcn_update_dpp(0, __builtin_bit_cast(int, v), CTRL, RM, 0xf, true)); }
DI float wave_sum(float v) {
  v = dpp_add<0x111, 0xf>(v); v = dpp_add<0x112, 0xf>(v); v = dpp_add<0x114, 0xf>(v); v = dpp_add<0x118, 0xf>(v);
  v = dpp_add<0x142, 0xa>(v); v = dpp_add<0x143, 0xc>(v);
  return __builtin_bit_cast(float, __builtin_amdgcn_readlane(__builtin_bit_cast(int, v), 63));
}
DI u32x4 ld_sc1(__amdgpu_buffer_rsrc_t rs, unsigned off) { return __builtin_amdgcn_raw_buffer_load_b128(rs, off, 0, 16); }
DI void st_sc1(__amdgpu_buffer_rsrc_t rs, unsigned off, u32x4 v) { __builtin_amdgcn_raw_buffer_store_b128(v, rs, off, 0, 16); }
#define SLA 2
#define SLZ 2
DI bool pair_ok(bool eq) { const unsigned long long m = __builtin_amdgcn_ballot_w64(eq); return (unsigned)(m | (m >> 32)) == ~0u; }
template <int N> DI void poll_flags(const unsigned char* ws, unsigned off, unsigned long long ep) {
  const int lane = threadIdx.x & 63;
  gu64* f = FLAG(off) + (lane & 31) + (lane >> 5) * (DUP - 1) * 1024;
#pragma unroll 1
  for (unsigned spins = 0; spins < SPIN_CAP; ++spins) {
    unsigned long long v[N];
    bool ok = true;
#pragma unroll
    for (int k = 0; k < N; ++k) v[k] = __hip_atomic_load(f + 32 * k, RLX_AGENT);
#pragma unroll
    for (int k = 0; k < N; ++k) ok &= pair_ok(v[k] == ep);
    if (ok) break;
    __builtin_amdgcn_s_sleep(N > 1 ? SLA : SLZ);
  }
  __builtin_amdgcn_fence(__ATOMIC_ACQUIRE, "wavefront");
}

#define PFC 1
DI unsigned long long next_epoch(unsigned long long e_in) {
  const unsigned hi = (unsigned)__builtin_amdgcn_readfirstlane((unsigned)(e_in >> 32)) + 0x9E3779B9u, lo = (unsigned)__builtin_amdgcn_readfirstlane((unsigned)e_in) + 0x7F4A7C15u;
  return ((unsigned long long)hi << 32) | lo;
}
constexpr int L_TROW = 3 * HTB;
#define LDT(base, q) (*(LAS const bf16x8*)((base) + (q) * 1024 + lane * 16))
#define LDTW(dst, base, q) do { _Pragma("unroll") for (int g_ = 0; g_ < 4; ++g_) { const f32x4 t_ = *(LAS const f32x4*)((base) + ((q) + g_) * 1024 + lane * 16); \
    dst[4 * g_] = t_[0]; dst[4 * g_ + 1] = t_[1]; dst[4 * g_ + 2] = t_[2]; dst[4 * g_ + 3] = t_[3]; } } while (0)
#define ROW(w, j) (2 * ((w) & 31) + ((j) >> 2) + 64 * (4 * ((w) >> 5) + ((j) & 3)))
template <int N0, int N1, int N2, class F>
DI unsigned long long row_fwd(F&& issue, LAS unsigned char* lds, const int scro, const u16* __restrict__ x,
                              unsigned char* __restrict__ ws, __amdgpu_buffer_rsrc_t rs, unsigned pf STAMP_ARG) {
  const int tid = otid(), lane = tid & 63, j = __builtin_amdgcn_readfirstlane(tid >> 6), w = blockIdx.x;
  const int r = lane & 31, h = lane >> 5;
  const int s = ROW(w, j);
  unsigned long long e_in;
  {
    const unsigned vz = 0;
    BAR;
    asm volatile("global_load_dwordx2 %0, %1, %2 sc1" : "=&v"(e_in) : "v"(vz), "s"(ws + WS_CTL) : "memory");
  }
  issue(0);
  LAS unsigned char* scr = lds + scro;
  LAS unsigned char* xt = scr + j * 2048;
  LAS const unsigned char* tb = lds + L_TROW;
  const int g2 = (lane >> 4) & 1, qq = (lane & 15) >> 2, pp = lane & 3, bz = (lane >> 1) & 1;
  int xo = (4 * h + qq) * 64 + g2 * 32 + pp * 8, b0 = r * 4 + h * 256 + (j >> 2) * 128;
  int to = bz * (8192 + 128) + j * 256 + (lane >> 2) * 8 + (lane & 1) * 4;
  unsigned so = WS_RB + w * 16384 + j * 2048 + lane * 16;
  float sgn = (lane & 1) ? -1.f : 1.f, dc = r == 0 ? -32.f : 0.f;
  PIN(xo); PIN(b0); PIN(to); PIN(so); PIN(sgn); PIN(dc);
  STAMPB(1);
  asm volatile("s_waitcnt vmcnt(%1)" : "+v"(pf) : "n"(N0 + 1) : "memory");
  BAR; FENCE_C;
  STAMPB(2);
  bf16x8 xa[2];
#pragma unroll
  for (int ks = 0; ks < 2; ++ks) xa[ks] = cat4(tr_read(xt + xo + ks * 1024), tr_read(xt + xo + ks * 1024 + 512));
  f32x16 tr = zero16(), ti = zero16();
#pragma unroll
  for (int ks = 0; ks < 2; ++ks) { tr = MFMA32(xa[ks], LDT(tb, ks), tr); ti = MFMA32(xa[ks], LDT(tb, 2 + ks), ti); }
  float twc[16], tws[16];
  LDTW(twc, tb, 8); LDTW(tws, tb, 12);
  bf16x8 a1[2], a2[2];
#pragma unroll
  for (int ks = 0; ks < 2; ++ks) { a1[ks] = LDT(tb, 4 + ks); a2[ks] = LDT(tb, 6 + ks); }
  SCHED; issue(1); SCHED;
  f32x2 v[8];
#pragma unroll
  for (int i = 0; i < 8; ++i) { const unsigned u = __builtin_bit_cast(u32x4, xa[i >> 2])[i & 3]; v[i] = (f32x2){bf_lo(u), bf_hi(u)}; }
  const f32x2 s2 = ((v[0] + v[1]) + (v[2] + v[3])) + ((v[4] + v[5]) + (v[6] + v[7]));
  const float ls = s2[0] + s2[1];
  const float mean = wave_sum(ls) * (1.f / 1024.f), alt = wave_sum(ls * sgn);
  const float mb = rbf(mean), dm = dc * mb;
  f32x16 ur, ui;
#pragma unroll
  for (int i = 0; i < 16; ++i) { ur[i] = __builtin_fmaf(tr[i], twc[i], __builtin_fmaf(ti[i], tws[i], dm)); ui[i] = ti[i] * twc[i] - tr[i] * tws[i]; }
  f32x16 o = zero16();
#pragma unroll
  for (int ks = 0; ks < 2; ++ks) { bf16x8 pr = pack_step(ur, ks), pi = pack_step(ui, ks); PIN(pr); PIN(pi); o = MFMA32(a1[ks], pr, o); o = MFMA32(a2[ks], pi, o); }
  SCHED; issue(2); SCHED;
  f32x2 q0 = {0.f, 0.f}, q1 = {0.f, 0.f};
#pragma unroll
  for (int i = 0; i < 4; ++i) { const f32x2 d0 = v[2 * i] - mean, d1 = v[2 * i + 1] - mean; q0 += d0 * d0; q1 += d1 * d1; }
  q0 += q1;
  const float var = wave_sum(q0[0] + q0[1]) * (1.f / 1024.f);
  const float den = rbf(__builtin_amdgcn_sqrtf(rbf(rbf(var) + 1e-5f))), rden = __builtin_amdgcn_rcpf(den);
  asm volatile("s_waitcnt vmcnt(%1)" : "+v"(e_in) : "n"(N0 + N1 + N2) : "memory");
  const unsigned long long ep = next_epoch(e_in);
  if (lane == 0) o[1] = alt;
#pragma unroll
  for (int q = 0; q < 8; ++q) *(LAS unsigned*)(xt + (b0 ^ ((q & 1) * 128)) + (q >> 1) * 512) = pack2(o[2 * q] * rden, o[2 * q + 1] * rden);
  WAIT_L0; BAR; SCHED;
  STAMPB(3);
  u32x4 c[2];
#pragma unroll
  for (int i = 0; i < 2; ++i) { LAS const unsigned* p = (LAS const unsigned*)(scr + (to ^ (i * 128))); c[i] = (u32x4){p[0], p[512], p[1024], p[1536]}; }
  if (lane == 0) __hip_atomic_store((gu64*)(ws + WS_STAT) + s, (ep & 0xffffffff00000000ull) | pack2(mb, den), RLX_AGENT);
#pragma unroll
  for (int i = 0; i < 2; ++i) st_sc1(rs, so + i * 1024, c[i]);
  return ep;
}

template <bool DA>
DI void cmm(f32x16& re, f32x16& im, const bf16x8 (&x)[4], const bf16x8 (&f)[8], const f32x16 r0, const f32x16 i0) {
  re = r0; im = i0;
#pragma unroll
  for (int ks = 0; ks < 4; ++ks) {
    if (DA) { re = MFMA32(x[ks], f[ks], re); im = MFMA32(x[ks], f[4 + ks], im); }
    else    { re = MFMA32(f[ks], x[ks], re); im = MFMA32(f[4 + ks], x[ks], im); }
  }
}
DI unsigned pack2s(float a, float b) { asm("" : "+v"(a), "+v"(b)); return pack2(a, b); }
#define MAD(a, b, c, d) __builtin_fmaf(a, b, (c) * (d))
#define BFLY(p, q, ar, ai, br, bi, c, s) do { const float dr = __builtin_fmaf(2.f, ar, -br), di = __builtin_fmaf(2.f, ai, -bi); \
    p = pack2s(MAD(br, c, bi, s), MAD(bi, c, -br, s)); q = pack2s(MAD(dr, c, di, s), MAD(di, c, -dr, s)); } while (0)
#define LDF(dst, q) do { _Pragma("unroll") for (int i_ = 0; i_ < 8; ++i_) dst[i_] = *(LAS const bf16x8*)(tl + ((q) + i_) * 1024); } while (0)

#define FS 22
#define TP 26
#define STR_(x) #x
#define STR(x) STR_(x)
#define PIPE 0
#define LASTB 0
#define PRE 2
#define OAUX 0
#define PRL 3
#define NB 2
constexpr int NU = 56 - FS;
#define UJ(u) ((u) < FS / 2 ? (u) : (u) < FS ? (u) - FS / 2 : (u) / 2)
#define UH(u) ((u) < FS ? (u) >= FS / 2 : (u) & 1)
struct P2 { int tm, tn, role, pn0, dir, wrap; };
DI P2 p2map() {
  int w = blockIdx.x; asm volatile("" : "+s"(w));
  const int xcd = w & 7, idx = w >> 3, g = xcd >> 1, ty = (g ^ (g >> 1)) & 1, dir = 1 - 2 * (g & 1);
  P2 p = {PMG(xcd) * 8 + (idx & 7), ty * 4 + (idx >> 3), g >> 1, (0x1b0e0d00 >> (8 * g)) & 31, dir, (0x00e41c00 << (24 - 8 * g)) >> 24};
  asm volatile("" : "+s"(p.pn0), "+s"(p.dir), "+s"(p.wrap));
  return p;
}
DI bool flags_ok(u32x2 f, unsigned long long ep, bool need) { return __all(!need || (f[0] == (unsigned)ep && f[1] == (unsigned)(ep >> 32))); }
#define LDFLAG(f, o) asm volatile("buffer_load_dwordx2 %0, %1, %2, 0 offen sc1" : "=&v"(f) : "v"(o), "s"(rs) : "memory")
DI unsigned flag_first(const P2& p2, int lane) { return WS_FH + ((p2.tm >> 1) + (lane >> 5) * (DUP - 1) * 8) * 1024 + ((blockIdx.x >> 1) & 3) * 128 + min(lane & 7, 6) * 8; }
struct Rest { f32x4 a[NB][4], cg[2], cu[2], svn; u32x4 o[2], xv[2]; unsigned so, ro; bool poll; };
DI void ld_x(Rest& R, const P2& p2, const u16* x, int wid, int lane) {
  R.ro = (p2.tm * 128 + (wid & 3) * 32 + (lane & 15)) * 2048 + p2.tn * 256 + p2.role * 128 + (wid >> 2) * 64 + (lane & 16) * 2 + (lane >> 5) * 16;
  asm volatile("global_load_dwordx4 %0, %2, %3\n\tglobal_load_dwordx4 %1, %2, %4" : "=&v"(R.xv[0]), "=&v"(R.xv[1]) : "v"(R.ro), "s"(x), "s"(x + 16384) : "memory");
}
#define WAITX(n) asm volatile("s_waitcnt vmcnt(%2)" : "+v"(R.xv[0]), "+v"(R.xv[1]) : "n"(n) : "memory")
DI unsigned w2_voff(int tid, int role) { const int row = tid >> 4, p = tid & 15; return row * 2048 + (((p >> 1) ^ (row & 7) ^ (role * 4)) * 32) + (p & 1) * 16; }
#define WFILL1(slot, so, voff, i) DMA16(rD, voff, (so) + (i) * 65536, ldsw, (slot) * 16384 + (i) * 8192)
#define WFILL(slot, so, voff) do { DMA16(rD, voff, (so), ldsw, (slot) * 16384); DMA16(rD, voff, (so) + 65536, ldsw, (slot) * 16384 + 8192); } while (0)
constexpr int L_TINV = K1_SCR + 16384;
#define TINV_FILL do { const __amdgpu_buffer_rsrc_t rT_ = MKRSRC(&g_tab, sizeof(Tabs)); const unsigned vo_ = otid() * 16; \
    DMA16(rT_, vo_, 48 * 1024, ldsw, L_TINV); DMA16(rT_, vo_, 56 * 1024, ldsw, L_TINV + 8192); } while (0)
constexpr int L_P8 = K1_SCR + 24576;
struct InvTab { bf16x8 b1[2], b2[2], ec[2], ens[2]; float twc[16], tws[16]; };
DI void inv_tab(InvTab& T, LAS const unsigned char* ti, int lane) {
#pragma unroll
  for (int ks = 0; ks < 2; ++ks) { T.ec[ks] = LDT(ti, ks); T.ens[ks] = LDT(ti, 2 + ks); T.b1[ks] = LDT(ti, 4 + ks); T.b2[ks] = LDT(ti, 6 + ks); }
  LDTW(T.twc, ti, 8); LDTW(T.tws, ti, 12);
}
DI unsigned long long fft_group(LAS unsigned char* smem, const u16* __restrict__ x, const u16* __restrict__ w_r, const u16* __restrict__ w_i, const u16* __restrict__ wd,
                                unsigned char* __restrict__ ws, __amdgpu_buffer_rsrc_t rs, Rest& R, unsigned pf STAMP_ARG) {
  const int wv = __builtin_amdgcn_readfirstlane(threadIdx.x >> 6);
  const int c = (blockIdx.x & 7) * 4 + (blockIdx.x >> 3) - 28;
  unsigned nw, no; float sc;
  const unsigned long long ep = row_fwd<5, 0, 0>([&](int q) {
    if (q) return;
    const __amdgpu_buffer_rsrc_t rT = MKRSRC(&g_tab, sizeof(Tabs));
    const unsigned lb = (unsigned)(size_t)smem + wv * 1024, vo = otid() * 16;
#pragma unroll
    for (int i = 0; i < 4; ++i) DMA16(rT, vo, 16384 + i * 8192, lb, K1_SCR + i * 8192);
    const int lane = otid() & 63, k = 512 * ((c >> 2) & 3) + 256 * (wv >> 2) + 64 * (c & 3) + 32 * (c >> 4) + (wv & 3) * 8 + (lane >> 3);
    const u16* pw = ((lane & 4) ? w_i : w_r) + ((lane & 2) ? -k & 2047 : k) * DF_ + (lane & 1) * 512;
    asm volatile("global_load_ushort %0, %1, off" : "=&v"(nw) : "v"(pw) : "memory");
    sc = (lane & 6) == 6 ? -0.125f : 0.125f;
    no = (lane & 2) * 0x3ffffffe + WS_NYQ + c * 1024 + wv * 128 + (lane >> 2) * 8 + (lane & 1) * 4;
    PIN(sc); PIN(no);
  }, smem, 65536, x, ws, rs, pf STAMP_PASS);
  {
    asm volatile("s_waitcnt vmcnt(3)" : "+v"(nw) :: "memory");
    const float t = dpp_add<0x4e, 0xf>(bf_lo(nw) * sc);
    const float o = __builtin_bit_cast(float, __builtin_amdgcn_update_dpp(0, __builtin_bit_cast(int, t), 0xb1, 0xf, 0xf, true));
    __builtin_amdgcn_raw_buffer_store_b32(__float_as_uint((no & 4) ? o - t : o + t), rs, no, 0, 16);
  }
  FENCE_C;
  STAMP(1); STAMPB(4);
  unsigned tl_ = (unsigned)(size_t)smem + K1_SCR + (threadIdx.x & 63) * 16; PIN(tl_);
  LAS const unsigned char* tl = (LAS const unsigned char*)(size_t)tl_;
  bf16x8 e0[8];
  unsigned dummy = 0;
  {
    const int lane = otid() & 63, half = lane & 1, row0 = 512 * (wv & 3) + 32 * (wv >> 2);
    const unsigned off = ((row0 + (lane >> 1)) * DF_ + 16 * c + half * 8 - ((lane >> 1) & 1)) * 2;
    u32x4 L[2][8], E;
#pragma unroll
    for (int n = 0; n < 8; ++n) L[0][n] = WLD((const u32x4a*)((const char*)w_r + off + n * (64 * DF_ * 2)));
    FENCE_C; WAIT_V(8); BAR;
    STAMPB(5);
    SETFLAG(threadIdx.x, WS_FA + blockIdx.x * 8, DUP);
#pragma unroll
    for (int n = 0; n < 8; ++n) L[1][n] = WLD((const u32x4a*)((const char*)w_i + off + n * (64 * DF_ * 2)));
    const u16* pe = ((lane & 2) ? w_i : w_r) + (row0 + ((lane >> 1) | 1) + 256 * half) * DF_ + 16 * c + 15;
#pragma unroll
    for (int n = 0; n < 4; ++n) E[n] = WLD((const unsigned*)(pe + n * 64 * DF_));
    SCHED;
    LDF(e0, 24);
    LAS unsigned char* wp = smem + half * 32768 + ((wv * 64 + (lane >> 1)) ^ (half * 4)) * 16;
#pragma unroll
    for (int part = 0; part < 2; ++part)
#pragma unroll
      for (int hh = 0; hh < 2; ++hh)
#pragma unroll
        for (int q = 0; q < 4; ++q) { u32x4 t = {L[part][4 * hh][q], L[part][4 * hh + 1][q], L[part][4 * hh + 2][q], L[part][4 * hh + 3][q]}; *(LAS u32x4*)(wp + part * 65536 + q * 8192 + hh * 512) = t; }
    WAIT_L0; BAR; FENCE_C;
    *(LAS u32x4*)(smem + L_P8 + wv * 1024 + half * 512 + (lane >> 1) * 16) = E;
  }
  STAMPB(6);
  {
    const int lane = otid() & 63, r = lane & 31, h = lane >> 5;
    if (wv == 0) {
      poll_flags<8>(ws, WS_FA, ep);
      if (c == 0 && lane == 0) __hip_atomic_store((gu64*)(ws + WS_CTL), ep, RLX_AGENT);
    }
    WAIT_L0; BAR; FENCE_C;
    STAMP(2);
    const int extra = c == 0 && wv == 0;
    const unsigned ro = WS_RB + (4 * (r >> 4) + 2 * ((r >> 2) & 1) + ((r >> 3) & 1) + 32 * h) * 16384 + (c * 8 + wv) * 64 + ((r & 3) ^ extra) * 16;
#define PRI 3
    if (PRI == 4 ? wv >= 4 : PRI && wv < 4) __builtin_amdgcn_s_setprio(2);
    LAS unsigned char* wi = smem + wv * 8192 + ((lane ^ (wv & 4)) * 16);
    const bool p8 = wv == 7 && (lane & 1);
    const int w1o = p8 ? L_P8 + (lane - 1) * 16 : (wv + (lane & 1)) * 8192 + ((lane ^ ((wv + (lane & 1)) & 4)) * 16);
    if (PRI == 2 && wv >= 4) __builtin_amdgcn_s_setprio(1);
    u32x4 qa[4], qb[4];
#pragma unroll 1
    for (int it = 0; it < 2; ++it) {
      const int col = it ^ extra;
      bf16x8 pa[4], pb[4], ef[8];
      f32x16 ar, ai, br, bi;
      const f32x16 z16 = zero16();
      if (it == 0) {
        u32x4 nx[2][2][4];
#pragma unroll
        for (int q = 0; q < 16; ++q) nx[q >> 3][(q >> 2) & 1][q & 3] = __builtin_amdgcn_raw_buffer_load_b128(rs, ro, ((q >> 3) * 8 + ((q >> 2) & 1) * 16 + (q & 3) * 64) * 16384, 16);
        FENCE_C;
#pragma unroll
        for (int g = 0; g < 2; ++g) {
          bf16x8 xa[4], xb[4];
#pragma unroll
          for (int ks = 0; ks < 4; ++ks) { xa[ks] = __builtin_bit_cast(bf16x8, nx[g][0][ks]); xb[ks] = __builtin_bit_cast(bf16x8, nx[g][1][ks]); }
          LDF(ef, 8);
          cmm<true>(ar, ai, xa, e0, z16, z16);
          if (g == 0) STAMP(3);
          cmm<true>(br, bi, xb, ef, ar, ai);
#pragma unroll
          for (int k = 0; k < 2; ++k) {
            const f32x4 tc = *(LAS const f32x4*)(tl + (2 * g + k) * 1024), ts = *(LAS const f32x4*)(tl + (4 + 2 * g + k) * 1024);
            u32x4 p, q;
#pragma unroll
            for (int n = 0; n < 4; ++n) {
              const int i = 8 * k + 2 * n;
              BFLY(p[n], q[n], ar[i], ai[i], br[i], bi[i], tc[n], ts[n]);
              BFLY(qa[2 * g + k][n], qb[2 * g + k][n], ar[i + 1], ai[i + 1], br[i + 1], bi[i + 1], tc[n], ts[n]);
            }
            pa[2 * g + k] = __builtin_bit_cast(bf16x8, p); pb[2 * g + k] = __builtin_bit_cast(bf16x8, q);
          }
          SCHED;
        }
        if (PFW) {
          const __amdgpu_buffer_rsrc_t rD = MKRSRC(wd, F_ * D_ * 2);
          const unsigned vo = lane < 56 ? (lane >> 3) * 2048 + (lane & 7) * 128 : 0x7ffffffcu;
          const unsigned so = ((((blockIdx.x >> 1) & 2) + (blockIdx.x & 1)) * 896 + ((c & 3) * 8 + wv) * 7) * 2048 + (p2map().tn >> 2) * 1024;
#pragma unroll
          for (int i = 0; i < 4; ++i) asm volatile("buffer_load_dword %0, %1, %2, %3 offen" : "+v"(dummy) : "v"(vo), "s"(rD), "s"(so + i * (224 * 2048)) : "memory");
        }
        if (PRI == 3 && wv >= 4) __builtin_amdgcn_s_setprio(3);
      } else {
        if (PRI == 2 && !extra) __builtin_amdgcn_s_setprio(0);
        if (PRI == 3 && wv >= 4) __builtin_amdgcn_s_setprio(0);
#pragma unroll
        for (int k = 0; k < 4; ++k) { pa[k] = __builtin_bit_cast(bf16x8, qa[k]); pb[k] = __builtin_bit_cast(bf16x8, qb[k]); }
      }
      int both = it & extra;
      if (!both) {
        LDF(ef, 16);
        cmm<false>(ar, ai, pa, e0, z16, z16);
        cmm<false>(br, bi, pb, ef, z16, z16);
        const unsigned sel = ((col ^ lane) & 1) ? 0x07060c0cu : 0x05040c0cu;
        LAS const unsigned char* wq = col ? smem + w1o : wi;
        LAS const unsigned char* wp = wq + (col && p8 ? 16 : 65536);
#pragma unroll
        for (int g = 0; g < 8; ++g) {
          const u32x4 fr = *(LAS const u32x4*)(wq + g * 1024), fi = *(LAS const u32x4*)(wp + g * 1024);
          u32x4 p;
#pragma unroll
          for (int n = 0; n < 4; ++n) {
            const float wr = __uint_as_float(__builtin_amdgcn_perm(fr[n], 0u, sel)), wi_ = __uint_as_float(__builtin_amdgcn_perm(fi[n], 0u, sel));
            const float xr = g < 4 ? ar[4 * g + n] : br[4 * (g & 3) + n], xi = g < 4 ? ai[4 * g + n] : bi[4 * (g & 3) + n];
            p[n] = pack2s(MAD(xr, wi_, xi, wr), MAD(xr, wr, -xi, wi_));
          }
          (g < 4 ? pa : pb)[g & 3] = __builtin_bit_cast(bf16x8, p);
        }
      }
      asm volatile("" : "+s"(both));
      if (both) {
        u32x4 pq[32];
        const int lane = otid(), r = lane & 31, h = lane >> 5;
#define LDPQ(q) pq[q] = __builtin_amdgcn_raw_buffer_load_b128(rs, lane * 16 + ((q) & 3) * 1024, WS_NYQ + ((q) >> 2) * 4096, 16)
#pragma unroll
        for (int q = 0; q < 16; ++q) LDPQ(q);
        SCHED;
        LDF(ef, 16);
        cmm<false>(ar, ai, pa, e0, z16, z16);
        cmm<false>(br, bi, pb, ef, z16, z16);
        unsigned aw[4], an[4];
#pragma unroll
        for (int i = 0; i < 4; ++i) {
          const int l = 256 * h + 32 * (i >> 1) + r;
          aw[i] = (unsigned)(size_t)smem + (i & 1) * 65536 + l * 4; an[i] = (unsigned)(size_t)smem + (~i & 1) * 65536 + (320 - l) * 4; PIN(aw[i]); PIN(an[i]);
        }
#define KO(q) (((q) >> 2 & 1) * 512 + ((q) & 3) * 64)
        FENCE_C;
#pragma unroll
        for (int q = 0; q < 32; ++q) { LAS float* p = (LAS float*)(size_t)aw[q >> 3] + KO(q); p[0] = (q < 16 ? ar : br)[q & 15]; p[1024] = (q < 16 ? ai : bi)[q & 15]; }
        FENCE_C; SCHED;
        float rv[32][2];
#define LDRV(q) do { LAS const float* p_ = (LAS const float*)(size_t)an[(q) >> 3] + 704 - KO(q); rv[q][0] = p_[0]; rv[q][1] = p_[1024]; } while (0)
#pragma unroll
        for (int q = 0; q < 8; ++q) LDRV(q);
#pragma unroll
        for (int g = 0; g < 8; ++g) {
          u32x4 p;
          SCHED;
#pragma unroll
          for (int n = 0; n < 4; ++n) {
            const int q = 4 * g + n;
            const float zr = (q < 16 ? ar : br)[q & 15], zi = (q < 16 ? ai : bi)[q & 15];
            const bool self = (q == 0 || q == 8) && lane == 0;
            const float rr = self ? zr : rv[q][0], ri = self ? zi : rv[q][1];
            const f32x4 f = __builtin_bit_cast(f32x4, pq[q]);
            p[n] = pack2s(__builtin_fmaf(rr, f[3], __builtin_fmaf(-ri, f[1], MAD(zr, f[2], zi, f[0]))), __builtin_fmaf(rr, f[1], __builtin_fmaf(ri, f[3], MAD(zr, f[0], -zi, f[2]))));
          }
          (g < 4 ? pa : pb)[g & 3] = __builtin_bit_cast(bf16x8, p);
          SCHED;
#pragma unroll
          for (int n = 0; n < 4; ++n) { if (g < 4) LDPQ(16 + 4 * g + n); if (g < 6) LDRV(8 + 4 * g + n); }
        }
#pragma unroll
        for (int q = 0; q < 4; ++q) asm volatile("" : "=v"(qa[q]), "=v"(qb[q]));
      }
      FENCE_C;
      LDF(ef, 8);
      cmm<true>(ai, ar, pa, e0, z16, z16);
      cmm<true>(bi, br, pb, ef, ai, ar);
#pragma unroll
      for (int k = 0; k < 4; ++k) {
        const f32x4 tc = *(LAS const f32x4*)(tl + k * 1024), ts = *(LAS const f32x4*)(tl + (4 + k) * 1024);
        u32x4 p, q;
#pragma unroll
        for (int n = 0; n < 4; ++n) BFLY(p[n], q[n], ai[4 * k + n], ar[4 * k + n], bi[4 * k + n], br[4 * k + n], tc[n], ts[n]);
        pa[k] = __builtin_bit_cast(bf16x8, p); pb[k] = __builtin_bit_cast(bf16x8, q);
      }
      LDF(ef, 16);
      cmm<false>(ai, ar, pa, e0, z16, z16);
      cmm<false>(bi, br, pb, ef, z16, z16);
      const unsigned zp = ((XP & 4) ? 0x70000000u : 0u) + WS_Z + (c * 16 + wv * 2 + col) * 8192 + lane * 16;
#pragma unroll
      for (int g = 0; g < 4; ++g) {
        u32x4 o0, o1;
#pragma unroll
        for (int n = 0; n < 4; ++n) { o0[n] = pack2(ar[4 * g + n], ai[4 * g + n]); o1[n] = pack2(br[4 * g + n], bi[4 * g + n]); }
        st_sc1(rs, zp + g * 2048, o0); st_sc1(rs, zp + g * 2048 + 1024, o1);
      }
      FENCE_C;
    }
  }
  STAMP(4);
#define PRV 0
  if (wv < 4) __builtin_amdgcn_s_setprio(0); else __builtin_amdgcn_s_setprio(PRV);
  WAIT_V(0); PIN(dummy); BAR;
  SETFLAG(threadIdx.x, WS_FZ + c * 8, DUP);
  {
    const P2 p2 = p2map();
    const __amdgpu_buffer_rsrc_t rD = MKRSRC(wd, F_ * D_ * 2);
    const unsigned ldsw = (unsigned)(size_t)smem + wv * 1024;
    const int lane = otid() & 63, r = lane & 31, h = lane >> 5;
    TINV_FILL;
    const int tok0 = 64 * (8 * (c >> 3) + 4 * ((c >> 1) & 1)) + 32 * ((c >> 2) & 1) + (c & 1) * 16 + wv;
    if (wv == 0) poll_flags<1>(ws, WS_FZ, ep);
    WAIT_V(0); BAR; FENCE_C;
    STAMP(5);
    u32x4 nyq[2];
    {
      u32x4 t[16];
#pragma unroll
      for (int i = 0; i < 16; ++i) t[i] = ld_sc1(rs, WS_Z + ((wv * 16 + i) * 4 + (lane >> 4)) * 8192 + c * 256 + (lane & 15) * 16);
#pragma unroll
      for (int o = 0; o < 2; ++o) nyq[o] = ld_sc1(rs, WS_Z + c * 256 + (wv + 8 * o) * 16);
#pragma unroll
      for (int i = 0; i < 16; ++i) { const int k2 = (wv * 16 + i) * 4 + (lane >> 4); *(LAS u32x4*)(smem + k2 * 256 + (((lane & 15) ^ (k2 & 15)) * 16)) = t[i]; }
    }
    InvTab T;
    inv_tab(T, smem + L_TINV, lane);
    WAIT_L0; BAR; SCHED;
    STAMP(6); STAMPT(1);
    float sg = h ? 0.f : (r & 1) ? -1.f : 1.f;
    unsigned za = (unsigned)(size_t)smem + (r + 256 * h) * 256 + ((wv ^ (r & 15)) * 16), yb = (unsigned)(size_t)smem + K1_SCR + wv * 2048;
    unsigned yw = yb + (((r * 4 + h * 2) ^ ((r >> 1) & 3)) * 16), yq = yb + ((lane ^ ((lane >> 3) & 3)) * 16), yo = WS_Y + tok0 * 2048 + lane * 16;
    unsigned vw = w2_voff(otid(), p2.role);
    const unsigned so = p2.pn0 * 262144 + p2.tn * 256;
    PIN(sg); PIN(nyq[0]); PIN(nyq[1]); PIN(za); PIN(yw); PIN(yq); PIN(yo); PIN(vw);
    u32x4 zc[8], a0, a1, vr, vi, yv[2];
    f32x16 ur[2], ui[2], y;
    float ta[8], tb[8];
#define BF(v) __builtin_bit_cast(bf16x8, v)
#define LDZ(o) do { _Pragma("unroll") for (int i = 0; i < 8; ++i) zc[i] = *(LAS const u32x4*)(size_t)((za ^ ((o) * 128)) + i * 8192); SCHED; } while (0)
#define GATH(m) do { a0 = (u32x4){zc[0][m], zc[1][m], zc[2][m], zc[3][m]}; a1 = (u32x4){zc[4][m], zc[5][m], zc[6][m], zc[7][m]}; PIN(a0); PIN(a1); SCHED; } while (0)
#define MA(n, k) do { if ((n) < 8) { f32x16& d = ((k) & 1) ? ui[(n) & 1] : ur[(n) & 1]; d = MFMA32(BF(((k) & 2) ? a1 : a0), (((k) & 1) ? T.b2 : T.b1)[(k) >> 1], (k) < 2 ? zero16() : d); PIN(d); SCHED; } } while (0)
#define MY(k) do { y = MFMA32(BF(((k) & 1) ? vi : vr), (((k) & 1) ? T.ens : T.ec)[(k) >> 1], (k) ? y : zero16()); PIN(y); SCHED; } while (0)
#define E8(...) do { _Pragma("unroll") for (int e = 0; e < 8; ++e) { __VA_ARGS__; asm volatile("" : "+v"(ta[e]), "+v"(tb[e])); } SCHED; } while (0)
#define TMUL(n, ks) E8(ta[e] = -ui[(n) & 1][8 * (ks) + e] * T.tws[8 * (ks) + e]; tb[e] = ur[(n) & 1][8 * (ks) + e] * T.tws[8 * (ks) + e])
#define TFMA(n, ks) E8(ta[e] = __builtin_fmaf(ur[(n) & 1][8 * (ks) + e], T.twc[8 * (ks) + e], ta[e]); tb[e] = __builtin_fmaf(ui[(n) & 1][8 * (ks) + e], T.twc[8 * (ks) + e], tb[e]))
#define TCVT do { _Pragma("unroll") for (int k = 0; k < 4; ++k) vr[k] = pack2(ta[2 * k], ta[2 * k + 1]); _Pragma("unroll") for (int k = 0; k < 4; ++k) vi[k] = pack2(tb[2 * k], tb[2 * k + 1]); PIN(vr); PIN(vi); SCHED; } while (0)
#define YWR do { *(LAS bf16x8*)(size_t)yw = pack_step(y, 0); *(LAS bf16x8*)(size_t)(yw ^ 16) = pack_step(y, 1); SCHED; } while (0)
#define YRD do { yv[0] = *(LAS const u32x4*)(size_t)yq; yv[1] = *(LAS const u32x4*)(size_t)(yq + 1024); SCHED; } while (0)
#define YST(n) do { _Pragma("unroll") for (int i = 0; i < 2; ++i) st_sc1(rs, yo + (64 * ((n) & 3) + 8 * ((n) >> 2)) * 2048 + i * 1024, yv[i]); SCHED; } while (0)
#define WF(n, k) do { if ((n) >= 4) { WFILL1(((n) - 4) * 2 + (k) / 2, so + (((n) - 4) * 2 + (k) / 2) * p2.dir * 262144, vw, (k) & 1); SCHED; } } while (0)
    LDZ(0); GATH(0); MA(0, 0); MA(0, 1); MA(0, 2); MA(0, 3);
#pragma unroll
    for (int n = 0; n < 8; ++n) {
      if (n < 7) GATH((n + 1) & 3);
      if (n == 2) LDZ(1);
      if (n == 4) { BAR; SCHED; STAMPT(3); }
      ur[n & 1][0] += bf_hi(nyq[n >> 2][n & 3]) * sg;
      MA(n + 1, 0); TMUL(n, 0); WF(n, 0); MA(n + 1, 1); TFMA(n, 0); MA(n + 1, 2);
      if (n) YWR;
      TCVT; MA(n + 1, 3);
      if (n) YRD;
      WF(n, 1); MY(0); TMUL(n, 1); WF(n, 2); MY(1); TFMA(n, 1);
      if (n) YST(n - 1);
      TCVT; MY(2); WF(n, 3); MY(3);
    }
    ld_x(R, p2, x, wv, lane);
    YWR; YRD; YST(7);
    STAMPT(5);
    R.poll = wv == 0;
    if (wv) WAIT_V(0);
    WAITX(2);
  }
  return ep;
}

#define POLH " sc1"
#define SLP0 0
#define SLP 6
#define HAUX 16
#define DOT4(acc, v, tagstr) do { const u32x4 u_ = __builtin_bit_cast(u32x4, v); \
    asm volatile("v_dot2c_f32_bf16 %0, %1, %2\n\tv_dot2c_f32_bf16 %0, %1, %3\n\tv_dot2c_f32_bf16 %0, %1, %4\n\tv_dot2c_f32_bf16 %0, %1, %5\n\ts_nop 2 ; " tagstr \
                 : "+v"(acc) : "s"(0x3f803f80u), "v"(u_[0]), "v"(u_[1]), "v"(u_[2]), "v"(u_[3])); } while (0)
#define LO2(v) __builtin_shufflevector(v, v, 0, 1)
#define HI2(v) __builtin_shufflevector(v, v, 2, 3)
DI u32x4 geglu(Rest& R, const f32x4 (&a)[4], LAS const unsigned char* sp, int i) {
  const f32x2 s01 = LO2(R.svn), s23 = HI2(R.svn);
  u32x4 o; f32x2 gs[4], us[4], e[4];
#define ST4(...) do { _Pragma("unroll") for (int q = 0; q < 4; ++q) { __VA_ARGS__; } SCHED; } while (0)
#define H2(v) ((q & 1) ? HI2(v) : LO2(v))
  ST4(asm("v_pk_fma_f32 %0, %1, %2, %3 op_sel_hi:[1,0,1]" : "=v"(gs[q]) : "v"(H2(R.cg[q >> 1])), "v"(s01), "v"(H2(a[q >> 1]))));
  ST4(asm("v_pk_mul_f32 %0, %1, %1" : "=v"(e[q]) : "v"(gs[q])));
  ST4(asm("v_pk_fma_f32 %0, %1, %2, %3 op_sel:[0,0,1] op_sel_hi:[1,0,1]" : "=v"(e[q]) : "v"(e[q]), "v"(s23), "v"(s01)));
  ST4(asm("v_pk_mul_f32 %0, %1, %2" : "=v"(e[q]) : "v"(e[q]), "v"(gs[q])));
  ST4(e[q][0] = __builtin_amdgcn_exp2f(e[q][0]); e[q][1] = __builtin_amdgcn_exp2f(e[q][1]));
  ST4(asm("v_pk_fma_f32 %0, %1, %2, %3 op_sel_hi:[1,0,1]" : "=v"(us[q]) : "v"(H2(R.cu[q >> 1])), "v"(s01), "v"(H2(a[2 + (q >> 1)]))));
  ST4(asm("v_pk_fma_f32 %0, %2, %1, %2 op_sel:[1,0,1] op_sel_hi:[1,1,1]" : "=v"(e[q]) : "v"(e[q]), "v"(s23)));
  if (i < 7) R.svn = *(LAS const f32x4*)(sp + (((i + 1) >> 2) * 128 + ((i + 1) & 3) * 16) * 16);
  ST4(e[q][0] = __builtin_amdgcn_rcpf(e[q][0]); e[q][1] = __builtin_amdgcn_rcpf(e[q][1]));
  ST4(asm("v_pk_mul_f32 %0, %1, %2" : "=v"(gs[q]) : "v"(us[q]), "v"(gs[q])));
  ST4(asm("v_pk_mul_f32 %0, %1, %2" : "=v"(gs[q]) : "v"(e[q]), "v"(gs[q])));
  ST4(o[q] = pack2(gs[q][0], gs[q][1]));
  return o;
}
#define HST(o, i) st_sc1(rs, R.so + ((i) >> 2) * (112 * 8192) + ((i) & 3) * 1024, o)
DI void geglu_rest(Rest& R, LAS const unsigned char* sp, __amdgpu_buffer_rsrc_t rs, bool late) {
  if (!late) { HST(R.o[0], 6 - NB); HST(R.o[1], 7 - NB); }
  u32x4 o[NB];
#pragma unroll
  for (int i = 0; i < NB; ++i) { o[i] = geglu(R, R.a[i], sp, 8 - NB + i); if (!late) HST(o[i], 8 - NB + i); }
  if (late) {
    HST(R.o[0], 6 - NB); HST(R.o[1], 7 - NB);
#pragma unroll
    for (int i = 0; i < NB; ++i) HST(o[i], 8 - NB + i);
  }
}
DI unsigned long long gemm1_group(LAS unsigned char* lds, const u16* __restrict__ x, const u16* __restrict__ w_r, const u16* __restrict__ w_i, const u16* __restrict__ wg, const u16* __restrict__ wu,
                    const u16* __restrict__ wd, unsigned char* __restrict__ ws, __amdgpu_buffer_rsrc_t rs, Rest& R, unsigned pf STAMP_ARG) {
  const int wid = __builtin_amdgcn_readfirstlane(threadIdx.x >> 6), wr = wid >> 2, ta = (wid >> 1) & 1, fb = wr * 2 + (wid & 1);
  const int w = blockIdx.x, xcd = w & 7, idx = w >> 3;
  const int pm = PMG(xcd) * 4 + (idx & 3), pn = (xcd >> 1) * 7 + (idx >> 2);
  const int rot = (pm >> 2) * 8, tcs = (pm & 3) * 2;
  unsigned voffA[2], voffB[2];
  const unsigned ldsw = (unsigned)(size_t)lds + (unsigned)wid * 1024u;
  const __amdgpu_buffer_rsrc_t rA = MKRSRC(x, S_ * D_ * 2), rG = MKRSRC(wg, D_ * F_ * 2), rU = MKRSRC(wu, D_ * F_ * 2), rD = MKRSRC(wd, F_ * D_ * 2);
  const unsigned cA = pm * 256 * D_ * 2, cB = pn * 128 * 2;
  constexpr unsigned hA = 128 * D_ * 2, kA = 128, kB = 64 * F_ * 2;
#define SA(b, h) (((b) * 2 + (h)) * HTB)
#define SB(b, h) ((4 + (b) * 2 + (h)) * HTB)
#define STAGE(bufoff, rsrc, soff, voff) do { DMA16P(rsrc, (voff)[0], soff, ldsw, (bufoff), POL_##rsrc); DMA16P(rsrc, (voff)[1], soff, ldsw, (bufoff) + 8192, POL_##rsrc); } while (0)
  f32x4 acc[2][2][4][2];
  int aoff0, aoff1, boff0, boff1;
  const unsigned a0 = cA + rot * kA, b0 = cB + rot * kB;
  const unsigned long long ep = row_fwd<4, 5, 2>([&](int q) {
    if (q == 0) {
      const int tid = otid(), lane = tid & 63, fr = lane & 15, fq = lane >> 4;
#pragma unroll
      for (int i = 0; i < 2; ++i) {
        const int b = tid * 16 + i * 8192;
        const int ra = b >> 7, ca = ((b >> 4) & 7) ^ ((ra >> 1) & 7);
        voffA[i] = (unsigned)(ra * D_ + ca * 8) * 2u;
        const int kb = b >> 8, p = (b >> 4) & 15, u = (p >> 1) ^ ((kb & 3) | (((kb >> 3) & 1) << 2));
        voffB[i] = (unsigned)(kb * F_ + u * 16 + (p & 1) * 8) * 2u;
      }
      STAGE(SB(0, 0), rG, b0, voffB); STAGE(SA(0, 0), rA, a0, voffA);
      SCHED;
      const int q4 = (lane & 15) >> 2, pp = lane & 3;
      aoff0 = (ta * 64 + fr) * 128 + ((fq ^ (fr >> 1)) * 16); aoff1 = aoff0 ^ 64;
      boff0 = (8 * fq + q4) * 256 + (((fb * 2) ^ (q4 | ((fq & 1) << 2))) * 32) + pp * 8; boff1 = boff0 ^ 32;
      PIN(aoff0); PIN(aoff1); PIN(boff0); PIN(boff1);
    } else if (q == 1) {
      STAGE(SB(0, 1), rU, b0, voffB); STAGE(SA(0, 1), rA, a0 + hA, voffA);
      const int gw = idx * 8 + wid, lane = otid() & 63, ln = (gw >= 112 ? gw - 112 : gw) * 37 + lane;
      const __amdgpu_buffer_rsrc_t rW = MKRSRC(gw >= 112 ? w_i : w_r, S_ * DF_ * 2);
      const unsigned vo = lane < 37 ? ((ln >> 1) * (DF_ * 2) + (xcd + (ln & 1)) * 128) & ~3u : 0x7ffffffcu;
      asm volatile("s_add_i32 m0, %0, %3\n\ts_nop 0\n\tbuffer_load_dword %1, %2, 0 offen" POLPF " lds" :: "s"(ldsw), "v"(vo), "s"(rW), "i"(K1_SCR + 16384) : "scc", "memory");
    }
    else STAGE(SB(1, 0), rG, b0 + kB, voffB);
    if (q != 1) {
#pragma unroll
      for (int i = q ? 24 : 0; i < (q ? 32 : 24); ++i) { acc[i >> 4][(i >> 3) & 1][(i >> 1) & 3][i & 1] = (f32x4){0.f, 0.f, 0.f, 0.f}; PIN(acc[i >> 4][(i >> 3) & 1][(i >> 1) & 3][i & 1]); }
    }
  }, lds, K1_SCR, x, ws, rs, pf STAMP_PASS);
  STAGE(SA(1, 0), rA, a0 + kA, voffA); STAGE(SB(1, 1), rU, b0 + kB, voffB);
  const unsigned tag = (unsigned)(ep >> 32);
  STAMP(1);
#define LDA(dst, b, h) LDA2(dst, b, h, 0, 2)
#define LDB(dst, b, h) LDB2(dst, b, h, 0, 2)
#define LDA2(dst, b, h, i0, i1) do { _Pragma("unroll") for (int m = 2 * (i0); m < 2 * (i1); ++m) { dst[m][0] = *(const LAS bf16x8*)(lds + SA(b, h) + aoff0 + m * 2048); dst[m][1] = *(const LAS bf16x8*)(lds + SA(b, h) + aoff1 + m * 2048); } } while (0)
#define LDB2(dst, b, h, i0, i1) do { _Pragma("unroll") for (int k = (i0); k < (i1); ++k) { \
    dst[0][k] = cat4(tr_read(lds + SB(b, h) + boff0 + k * 8192), tr_read(lds + SB(b, h) + boff0 + k * 8192 + 1024)); \
    dst[1][k] = cat4(tr_read(lds + SB(b, h) + boff1 + k * 8192), tr_read(lds + SB(b, h) + boff1 + k * 8192 + 1024)); } } while (0)
#define MO 0
#define ORD 5
#define OM(i) (MO == 1 ? (((i) >> 1) & 3) ^ (((i) >> 3) * 3) : MO == 2 ? ((i) & 3) ^ ((((i) >> 2) & 1) * 3) : MO == 3 ? (i) >> 2 : MO == 4 ? (((i) >> 1) & 3) ^ (((i) >> 3) * 3) : ((i) >> 1) & 3)
#define ON(i) (MO == 1 ? (((i) + 1) >> 1) & 1 : MO == 2 ? ((((i) + 4) >> 3) & 1) : MO == 3 ? ((i) >> 1) & 1 : MO == 4 ? (i) >> 3 : (i) & 1)
#define OK(i) (MO == 3 ? (((i) + 1) >> 1) & 1 : MO == 4 ? (((i) + 1) >> 1) & 1 : (i) >> 3)
#define MMA(ai, bj, At, Bt) MMX(ai, bj, At, Bt, (void)0)
#define MMX(ai, bj, At, Bt, ex) do { SCHED; __builtin_amdgcn_s_setprio(1); _Pragma("unroll") for (int i = 0; i < 16; ++i) { \
    acc[ai][bj][OM(i)][ON(i)] = MFMA16(Bt[ON(i)][OK(i)], At[OM(i)][OK(i)], acc[ai][bj][OM(i)][ON(i)]); PIN(acc[ai][bj][OM(i)][ON(i)]); SCHED; if (i == DK) { ex; SCHED; } } __builtin_amdgcn_s_setprio(0); SCHED; } while (0)
#define MMB(ai, bj, At, Bt, ...) do { MMX(ai, bj, At, Bt, if (ORD == 4) DMA1(1, __VA_ARGS__)); BAR; } while (0)
#define DMA1(i, bufoff, rsrc, soff, voff) DMA16P(rsrc, (voff)[i], soff, ldsw, (bufoff) + (i) * 8192, POL_##rsrc)
#define DK 8
#define LOADS(rd, dst, b, h, ...) do { SCHED; if (ORD >= 1 && ORD != 5) DMA1(0, __VA_ARGS__); if (ORD == 1) DMA1(1, __VA_ARGS__); SCHED; rd(dst, b, h, 0, 1); SCHED; if (ORD == 3) DMA1(1, __VA_ARGS__); if (ORD == 5) DMA1(0, __VA_ARGS__); \
    SCHED; rd(dst, b, h, 1, 2); SCHED; if (ORD == 0) DMA1(0, __VA_ARGS__); if (ORD == 0 || ORD == 2 || ORD == 5) DMA1(1, __VA_ARGS__); } while (0)
#define WAIT_VL(n) asm volatile("s_waitcnt vmcnt(%0)" :: "n"((n) - (ORD == 4)) : "memory")
#define CSUM do { if (ta == 0) { DOT4(cs0, B0[0][0], "g"); DOT4(cs1, B0[1][0], "g"); DOT4(cs0, B0[0][1], "g"); DOT4(cs1, B0[1][1], "g"); } \
    else { DOT4(cs0, B1[0][0], "u"); DOT4(cs1, B1[1][0], "u"); DOT4(cs0, B1[0][1], "u"); DOT4(cs1, B1[1][1], "u"); } } while (0)
  bf16x8 At[4][2], B0[2][2], B1[2][2];
  constexpr int nt = D_ / 64;
  WAIT_V(10); BAR;
  if (wr == 1) BAR;
  STAMP(2); STAMPB(4);
#define RARE(c) __builtin_expect_with_probability(!!(c), 0, 0.999)
  LDB(B0, 0, 0);
  for (int t = 0; t < nt - 2; t += 2) {
    const unsigned k2 = (t + 2 + rot) & 15;
    const unsigned a1 = cA + (((t + rot) & 15) + 1) * kA, a2 = cA + k2 * kA, a3 = a2 + kA, b2 = cB + k2 * kB, b3 = b2 + kB;
    float cs0 = 0.f, cs1 = 0.f;
    LOADS(LDA2, At, 0, 0, SA(1, 1), rA, a1 + hA, voffA);
    WAIT_L(8); BAR; WAIT_L0; MMB(0, 0, At, B0, SA(1, 1), rA, a1 + hA, voffA); SCHED;
    LOADS(LDB2, B1, 0, 1, SB(0, 0), rG, b2, voffB);
    BAR; WAIT_L0; MMB(0, 1, At, B1, SB(0, 0), rG, b2, voffB);
    LOADS(LDA2, At, 0, 1, SA(0, 0), rA, a2, voffA);
    WAIT_VL(10);
    BAR; WAIT_L0; MMB(1, 0, At, B0, SA(0, 0), rA, a2, voffA); SCHED;
    if (RARE(t == tcs)) CSUM;
    LOADS(LDB2, B0, 1, 0, SB(0, 1), rU, b2, voffB);
    WAIT_VL(6);
#define FAE 0
    if (FAE && RARE(t + wid == 0)) SETFLAG(otid(), WS_FA + w * 8, DUP);
    BAR; MMB(1, 1, At, B1, SB(0, 1), rU, b2, voffB);
    if (!FAE && RARE(t + wid == 0)) SETFLAG(otid(), WS_FA + w * 8, DUP);
    if (t == 0) STAMPB(5);
    if (t == 2) STAMPB(6);
    u32x2 gr[5];
    if (RARE(t == nt - 4)) {
      const unsigned go = (otid() & 255) * 8, so = WS_CS + (pn * 8 + wr * 4) * 2048;
      asm volatile("buffer_load_dwordx2 %0, %5, %6, %7 offen sc1\n\tbuffer_load_dwordx2 %1, %5, %6, %7 offen offset:2048 sc1\n\tbuffer_load_dwordx2 %2, %5, %6, %8 offen sc1\n\t"
                   "buffer_load_dwordx2 %3, %5, %6, %8 offen offset:2048 sc1\n\tbuffer_load_dwordx2 %4, %5, %6, %9 offen sc1"
                   : "=&v"(gr[0]), "=&v"(gr[1]), "=&v"(gr[2]), "=&v"(gr[3]), "=&v"(gr[4]) : "v"(go), "s"(rs), "s"(so), "s"(so + 4096), "s"(WS_STAT + pm * 2048) : "memory");
    }
    LOADS(LDA2, At, 1, 0, SA(0, 1), rA, a2 + hA, voffA);
    WAIT_L(8); BAR; WAIT_L0; MMB(0, 0, At, B0, SA(0, 1), rA, a2 + hA, voffA); SCHED;
    LOADS(LDB2, B1, 1, 1, SB(1, 0), rG, b3, voffB);
    BAR; WAIT_L0; MMB(0, 1, At, B1, SB(1, 0), rG, b3, voffB);
    LOADS(LDA2, At, 1, 1, SA(1, 0), rA, a3, voffA);
    WAIT_VL(10);
    BAR; WAIT_L0; MMB(1, 0, At, B0, SA(1, 0), rA, a3, voffA); SCHED;
    if (RARE(t == tcs)) CSUM;
    LOADS(LDB2, B0, 0, 0, SB(1, 1), rU, b3, voffB);
    WAIT_VL(6);
    if (RARE(t == tcs)) {
      const u32x2 p = __builtin_amdgcn_permlane16_swap(__float_as_uint(cs0), __float_as_uint(cs1), false, false);
      const unsigned e = __float_as_uint(__uint_as_float(p[0]) + __uint_as_float(p[1]));
      const u32x2 q = __builtin_amdgcn_permlane32_swap(e, e, false, false);
      const unsigned c = __float_as_uint(__uint_as_float(q[0]) + __uint_as_float(q[1]));
      const int lane = otid() & 63;
      if (lane < 32) __hip_atomic_store((gu64*)(ws + WS_CS) + (pn * 8 + pm) * 256 + ta * 128 + fb * 32 + lane, ((unsigned long long)tag << 32) | c, RLX_AGENT);
    }
    if (RARE(t == nt - 4)) {
      asm volatile("" : "+v"(gr[0]), "+v"(gr[1]), "+v"(gr[2]), "+v"(gr[3]), "+v"(gr[4]) :: "memory");
#pragma unroll 1
      for (unsigned spins = 0; spins < SPIN_CAP; ++spins) {
        bool ok = true;
#pragma unroll
        for (int i = 0; i < 5; ++i) ok &= gr[i][1] == tag;
        if (__all(ok)) break;
        __builtin_amdgcn_s_sleep(2); FENCE_C;
        const unsigned go = (otid() & 255) * 8;
#pragma unroll
        for (int i = 0; i < 4; ++i) gr[i] = __builtin_amdgcn_raw_buffer_load_b64(rs, go + i * 2048, WS_CS + (pn * 8 + wr * 4) * 2048, 16);
        gr[4] = __builtin_amdgcn_raw_buffer_load_b64(rs, go, WS_STAT + pm * 2048, 16);
        asm volatile("" : "+v"(gr[0]), "+v"(gr[1]), "+v"(gr[2]), "+v"(gr[3]), "+v"(gr[4]) :: "memory");
      }
      const int tid = otid();
      *(LAS float*)(lds + K1_SCR + tid * 4) = (__uint_as_float(gr[0][0]) + __uint_as_float(gr[1][0])) + (__uint_as_float(gr[2][0]) + __uint_as_float(gr[3][0]));
      if (tid == 511) *(LAS unsigned*)(lds + K1_SCR + 8192) = 0;
      if (wr == 0) {
        const float den = bf_hi(gr[4][0]), rd = __builtin_amdgcn_rcpf(den), k1 = -2.f * 0.7978845608028654f * 1.4426950408889634f;
        *(LAS f32x4*)(lds + K1_SCR + 2048 + tid * 16) = (f32x4){-bf_lo(gr[4][0]), k1 * rd, k1 * 0.044715f * rd * rd * rd, den * den};
      }
    }
    BAR; MMB(1, 1, At, B1, SB(1, 1), rU, b3, voffB);
  }
  STAMP(3);
  const P2 p2 = p2map();
  const unsigned so2 = p2.pn0 * 262144 + p2.tn * 256, dw = p2.dir * 262144;
  { LDA(At, 0, 0); STAGE(SA(1, 1), rA, cA + (((nt - 2 + rot) & 15) + 1) * kA + hA, voffA);
    unsigned vw = w2_voff(otid(), p2.role); PIN(vw);
    BAR; WAIT_L0; MMA(0, 0, At, B0); BAR;
    LDB(B1, 0, 1); BAR; WAIT_L0; MMA(0, 1, At, B1); BAR;
    LDA(At, 0, 1); WFILL(0, so2, vw); WFILL(4, so2 + 4 * dw, vw); WAIT_V(6); BAR; WAIT_L0; MMA(1, 0, At, B0); MMA(1, 1, At, B1); BAR;
    LDB(B0, 1, 0); LDA(At, 1, 0); WFILL(5, so2 + 5 * dw, vw); BAR; WAIT_L0; MMA(0, 0, At, B0); BAR;
    LDB(B1, 1, 1); WFILL(1, so2 + dw, vw); WAIT_V(8); BAR; WAIT_L0; MMA(0, 1, At, B1); BAR;
    LDA(At, 1, 1); WFILL(2, so2 + 2 * dw, vw); WFILL(6, so2 + 6 * dw, vw);
    WAIT_L(0); BAR; MMA(1, 0, At, B0); MMA(1, 1, At, B1);
    if (LASTB) BAR;
    if (wr == 0) { BAR; __builtin_amdgcn_s_setprio(PRE); }
    WFILL(3, so2 + 3 * dw, vw); WFILL(7, so2 + 7 * dw, vw); }
  STAMP(4);
  const int lane = otid() & 63, fr = lane & 15, fq = lane >> 4;
  ld_x(R, p2, x, wid, lane);
#pragma unroll
  for (int n = 0; n < 2; ++n) {
    LAS const unsigned char* p = lds + K1_SCR + (fb * 32 + n * 16 + fq * 4) * 4;
    R.cg[n] = *(LAS const f32x4*)p + *(LAS const f32x4*)(p + 1024); R.cu[n] = *(LAS const f32x4*)(p + 512) + *(LAS const f32x4*)(p + 1536);
  }
  LAS const unsigned char* sp = lds + K1_SCR + 2048 + (ta * 64 + fr) * 16;
  R.svn = *(LAS const f32x4*)sp;
  R.so = WS_H + ((pm * 224 + pn * 4 + fb) * 8 + ta * 4) * 1024 + lane * 16; R.poll = false;
#define SLL 0
  if (SLL && wr) __builtin_amdgcn_s_sleep(SLL);
#pragma unroll
  for (int i = 0; i < 8 - NB; ++i) {
    const f32x4 a[4] = {acc[i >> 2][0][i & 3][0], acc[i >> 2][0][i & 3][1], acc[i >> 2][1][i & 3][0], acc[i >> 2][1][i & 3][1]};
    const u32x4 o = geglu(R, a, sp, i);
    if (i < 6 - NB) HST(o, i); else R.o[i - 6 + NB] = o;
  }
#pragma unroll
  for (int i = 0; i < 4 * NB; ++i) R.a[i >> 2][i & 3] = acc[1][(i >> 1) & 1][4 - NB + (i >> 2)][i & 1];
  WAITX(6 - NB);
  if (wr == 0) {
    geglu_rest(R, sp, rs, false);
    WAIT_V(0);
    unsigned n = 0;
    if (lane == 0) n = __hip_atomic_fetch_add((LAS unsigned*)(lds + K1_SCR + 8192), 1u, __ATOMIC_RELAXED, __HIP_MEMORY_SCOPE_WORKGROUP);
    R.poll = __builtin_amdgcn_readfirstlane(n) == 3;
    if (R.poll) { SETFLAG(lane, WS_FH + pm * 1024 + (xcd >> 1) * 128 + (idx >> 2) * 8, DUP); __builtin_amdgcn_s_sleep(SLP0); }
  }
  STAMP(5);
  return ep;
#undef SA
#undef SB
#undef STAGE
#undef LDA
#undef LDB
#undef MMA
}

DI u32x2 ld_flag(__amdgpu_buffer_rsrc_t rs, unsigned off) { return __builtin_amdgcn_raw_buffer_load_b64(rs, off, 0, 16); }
template <bool PAIR> DI void spin_flags(__amdgpu_buffer_rsrc_t rs, unsigned off, unsigned long long ep, bool need) {
#pragma unroll 1
  for (unsigned spins = 0; spins < SPIN_CAP; ++spins) {
    __builtin_amdgcn_s_sleep(SLP); FENCE_C;
    const u32x2 f = ld_flag(rs, off);
    if (PAIR ? pair_ok(f[0] == (unsigned)ep && f[1] == (unsigned)(ep >> 32)) : flags_ok(f, ep, need)) break;
  }
}
#define NI 3
#define RN 9
#define RD 2
#define TL 2
#define TY 26
#define FLB 0
#define FLC 4
#define FLD 7
constexpr int OP_H = 1, OP_W = 200, OP_F = 300, OP_S = 310, OP_Y = 320, OP_P = 330;
constexpr int FL[3] = {FLB, FLC, FLD}, FC[3] = {FLB + 2, FLC + 2, 12}, FN[5] = {7, FS / 2, FS, NU + 2, 64};
constexpr int ustep(int u) { return u < FS ? u : (u + FS) / 2; }
struct Sched {
  short op[28][8]; int w[28]; bool ok;
  constexpr Sched() : op(), w(), ok(true) {
    int at[340] = {}, n = 0, nh = 0, nw = 16, ns = 0, ny = 0, np = 0, nf = 0, done = 0;
    while (nh < 2 * NI) at[OP_H + nh++] = ++n;
    const int n0 = n;
    for (int t = 0; t < 28; ++t) {
      int need = t == TL ? n0 : 0, seen = 0;
      const int rq[6] = {OP_H + (t < FS ? 2 * t + 1 : 4 * t - 2 * FS + 3), !(t & 1) && t >= 6 ? OP_W + 2 * (t < 26 ? t + 2 : 27) + 1 : 0, t == FC[0] ? OP_F : 0, t == FC[1] ? OP_F + 1 : 0,
                         t == FC[2] ? OP_F + 2 : 0, t == TY ? OP_Y + 1 : 0};
      for (int i = 0; i < 6; ++i) if (rq[i]) { ok &= at[rq[i]] > 0; if (at[rq[i]] > need) need = at[rq[i]]; }
      w[t] = need > done ? n - need : 63;
      if (need > done) done = need;
      for (int i = 0; i < 3; ++i) seen += t >= FC[i];
      int fix = 0;
      for (int i = 0; i < 3; ++i) if (t == FL[i]) fix = OP_F + i;
      if (fix) ++nf;
      const int b = n0 + nf + (t + 1) * RN / RD - n, mask = b <= 4 ? 0xaa : b == 5 ? 0xab : 0xbb;
      for (int k = 0; k < 8; ++k) {
        if (!((mask >> k) & 1)) continue;
        const int u = nh / 2, p = ustep(u - 8), c = nw / 2;
        const bool h = u < NU && u < FN[seen] && (u < 8 || p < t || (p == t && k >= 3 + 4 * (nh & 1) - (t >= FS && !(u & 1) ? 2 : 0)));
        const bool d = c < 28 && t >= 2 && t >= ((c - 7) & ~1);
        const bool y = ny < 2 && seen == 3;
        int o = 0;
        if (fix) { o = fix; fix = 0; }
        else if (ns < 4 && t * 8 + k >= (FS + 1) * 8 + 2 + ns) o = OP_S + ns++;
        else if (y && (!h || TY < ustep(u)) && (!d || TY < ((c - 1) & ~1))) o = OP_Y + ny++;
        else if (d && (!h || ((c - 1) & ~1) <= ustep(u))) o = OP_W + nw++;
        else if (h) o = OP_H + nh++;
        else if (t >= TP && np < 4) o = OP_P + np++;
        if (o) at[op[t][k] = o] = ++n;
      }
    }
    ok &= np == 4;
  }
};
constexpr Sched SC;
static_assert(SC.ok, "");
DI void phase2(LAS unsigned char* lds, const u16* __restrict__ x, const u16* __restrict__ wd, unsigned char* __restrict__ ws, u16* __restrict__ out,
               __amdgpu_buffer_rsrc_t rs, const unsigned long long ep, Rest& R STAMP_ARG) {
  const int wid = __builtin_amdgcn_readfirstlane(threadIdx.x >> 6), tg = wid & 3, kg = wid >> 2;
  const int w = blockIdx.x, xcd = w & 7, idx = w >> 3;
  const P2 p2 = p2map();
  const __amdgpu_buffer_rsrc_t rD = MKRSRC(wd, F_ * D_ * 2);
  const unsigned ldsw = (unsigned)(size_t)lds + wid * 1024;
  LAS unsigned char* scr = lds + K1_SCR;
  u32x4 H[8][2], rv[2], pv[4];
  u32x4 (&xv)[2] = R.xv;
  const unsigned ro = R.ro;
  u32x2 q[4];
  f32x4 acc[4][4];
  bf16x8 Wa[8], Wb[8];
  unsigned vh, vw, wa[8], fo;
  const unsigned dh = (XP & 64) ? 0 : p2.dir * 32768, dw = (XP & 128) ? 0 : p2.dir * 262144;
  unsigned sh = WS_H + p2.tm * (112 * 8192) + p2.pn0 * 32768, sw = (p2.pn0 + 7 * p2.dir) * 262144 + p2.tn * 256;
#define LP(a) ((LAS const unsigned char*)(size_t)(a))
#define PINS(v) asm volatile("" : "+s"(v))
#define HLD(u, m) do { if ((u) && !(m)) { sh += (UJ(u) - UJ((u) - 1)) * dh + (UH(u) - UH((u) - 1)) * 16384 + ((u) == 28 && !(XP & 64) ? p2.wrap * 32768 : 0); PINS(sh); } \
    asm volatile("buffer_load_dwordx4 %0, %1, %2, %3 offen offset:%4 sc1" : "=&v"(H[(u) & 7][m]) : "v"(vh), "s"(rs), "s"(sh), "n"((m) * 1024) : "memory"); } while (0)
#define WRD(D, n, slot) do { D[n] = cat4(tr_read(LP(wa[n]) + ((slot) & 3) * 16384), tr_read(LP(wa[n]) + ((slot) & 3) * 16384 + 4096)); SCHED; } while (0)
  {
    const int tid = otid(), lane = tid & 63;
#pragma unroll
    for (int n = 0; n < 8; ++n) { wa[n] = (unsigned)(size_t)lds + (kg * 32 + (lane >> 2)) * 256 + ((n ^ (2 * kg) ^ ((lane >> 2) & 7)) * 32) + (lane & 3) * 8; PIN(wa[n]); }
    vh = (kg * 8 + tg * 2) * 1024 + lane * 16;
    PIN(vh);
    if (!PIPE) { if (R.poll) spin_flags<true>(rs, flag_first(p2, lane), ep, true); }
    else {
      asm volatile("" : "=v"(q[0]), "=v"(q[1]), "=v"(q[2]), "=v"(q[3]));
      if (R.poll) {
        const unsigned ff = flag_first(p2, lane);
#define POLL(i) asm volatile("buffer_load_dwordx2 %0, %1, %2, 0 offen sc1\n\ts_sleep " STR(PIPE) : "+v"(q[i]) : "v"(ff), "s"(rs) : "memory")
#define SEEN(i) asm volatile("s_waitcnt vmcnt(3)" : "+v"(q[i]) :: "memory"); if (pair_ok(q[i][0] == (unsigned)ep && q[i][1] == (unsigned)(ep >> 32))) break; POLL(i)
        POLL(0); POLL(1); POLL(2); POLL(3);
#pragma unroll 1
        for (unsigned spins = 0; spins < SPIN_CAP; ++spins) { SEEN(0); SEEN(1); SEEN(2); SEEN(3); }
      }
    }
    if (wid >= 4) __builtin_amdgcn_s_setprio(PRL); else __builtin_amdgcn_s_setprio(0);
    STAMP(1);
    BAR; FENCE_C;
    STAMP(2);
    if (w >= 224) SETFLAG(tid, WS_FY + (xcd * 4 + idx - 28) * 8, 1);
#pragma unroll
    for (int u = 0; u < 2 * NI; ++u) HLD(u >> 1, u & 1);
#pragma unroll
    for (int n = 0; n < 8; ++n) WRD(Wa, n, 0);
    vw = w2_voff(tid, p2.role);
    const int j = UJ(lane), pn = p2.pn0 + p2.dir * j + (j < 14 ? 0 : p2.wrap);
    fo = lane < NU ? WS_FH + (p2.tm >> 1) * 1024 + UH(lane) * 512 + (pn + 9 * ((pn * 37) >> 8)) * 8
                   : WS_FY + ((p2.tm >> 2) * 8 + (tg & 1) * 4 + (p2.tm & 2) + (lane & 1)) * 8;
    PIN(vw); PIN(fo);
    if (wid >= 4) {
      if (w < 224) geglu_rest(R, scr + 2048 + ((tg >> 1) * 64 + (lane & 15)) * 16, rs, true);
      __builtin_amdgcn_s_setprio(0);
    }
    FENCE_C; SCHED;
    STAMP(3);
  }
  u32x2 f;
  f32x4 t[4];
  float rf[16];
  unsigned lx;
  const unsigned tag = (unsigned)(ep >> 32), so = WS_P + (w * 8 + wid) * 4096;
#define WTOG do { _Pragma("unroll") for (int n = 0; n < 8; ++n) { wa[n] ^= 65536; PIN(wa[n]); } } while (0)
#define XW(n) (*(LAS f32x4*)(scr + wid * 4096 + lx + (n) * 1024))
#define XQ(n) (*(LAS const f32x4*)(scr + (wid ^ 4) * 4096 + lx + (n) * 1024))
#define WRX(a) do { XW(0) = a[0]; XW(1) = a[1]; XW(2) = a[2]; XW(3) = a[3]; } while (0)
#define RES(k) do { if (T >= TY && (k) < 8 / (28 - TY)) { constexpr int n = ((T - TY) * (8 / (28 - TY)) + (k)) & 7; \
    const unsigned u = pack2s(bf_lo(xv[n >> 2][n & 3]) + bf_lo(rv[n >> 2][n & 3]), bf_hi(xv[n >> 2][n & 3]) + bf_hi(rv[n >> 2][n & 3])); \
    rf[2 * n] = bf_lo(u); rf[2 * n + 1] = bf_hi(u); PIN(rf[2 * n]); PIN(rf[2 * n + 1]); } } while (0)
#define ADD4(k) do { if (T == FS + 1) { const u32x4 g = {pack2s(acc[2][k][0] + t[k][0], acc[2][k][1] + t[k][1]), tag, pack2s(acc[2][k][2] + t[k][2], acc[2][k][3] + t[k][3]), tag}; \
    t[k] = __builtin_bit_cast(f32x4, g); PIN(t[k]); } } while (0)
#define LDP(n) asm volatile("buffer_load_dwordx4 %0, %1, %2, %3 offen offset:%4 sc1" : "=&v"(pv[n]) : "v"(lx), "s"(rs), "s"(so ^ (6 * 32768)), "n"((n) * 1024) : "memory")
#define ST1 4
#define ST2 8
#define ST3 16
#define ST4_ 20
#define ST5 24
#define FMASK(i) ((unsigned)((otid() & 63) - FN[i]) < (unsigned)(FN[(i) + 1] - FN[i]))
#define OP(k) do { constexpr int o = SC.op[T][k]; \
    if (o >= OP_P) LDP((o - OP_P) & 3); \
    else if (o >= OP_Y) asm volatile("buffer_load_dwordx4 %0, %1, %2, %3 offen sc1" : "=&v"(rv[o & 1]) : "v"(ro), "s"(rs), "s"(WS_Y + (o & 1) * 32768) : "memory"); \
    else if (o >= OP_S) st_sc1(rs, ((XP & 8) ? 0x70000000u : 0u) + so + ((o - OP_S) & 3) * 1024 + lx, __builtin_bit_cast(u32x4, t[(o - OP_S) & 3])); \
    else if (o >= OP_F) LDFLAG(f, FMASK((o - OP_F) & 3) ? fo : 0x7ffffff8u); \
    else if (o >= OP_W) { constexpr int c = (o - OP_W) / 2, u1 = c < FS ? c : 2 * c - FS, u0 = c <= FS ? c - 1 : u1 - 2; \
      if (o == OP_W + 2 * FS) { const int tid = otid(), row = tid >> 4, lu = ((tid >> 1) & 7) ^ (row & 7); vw = row * 2048 + (lu >> 2) * 131072 + (lu & 3) * 32 + (tid & 1) * 16; sw += p2.role * 128; } \
      if (!(o & 1)) { sw += (UJ(u1) - UJ(u0)) * dw + (UH(u1) - UH(u0)) * 131072 + (u1 == 28 && !(XP & 128) ? p2.wrap * 262144 : 0); PINS(sw); } \
      WFILL1(c & 7, sw, vw, o & 1); } \
    else if (o) HLD(((o - OP_H) >> 1) & 63, (o - OP_H) & 1); \
    SCHED; } while (0)
#define MM1(n, s, j) do { constexpr int i_ = ((n) >> 1) & (T < FS ? 3 : 1), q_ = (j) * 2 + ((n) & 1); \
    acc[i_][q_] = MFMA16(C[((n) & 6) + (j)], __builtin_bit_cast(bf16x8, H[s][(n) & 1]), T == 0 ? (f32x4)(0.f) : acc[i_][q_]); PIN(acc[i_][q_]); SCHED; } while (0)
#define MM(n, s) do { MM1(n, s, 0); MM1(n, s, 1); } while (0)
#define NR(i) (T + 1 < FS ? (i) : (i) ^ 2)
#define WRD_(n) do { if (T < 27) WRD(D, n, T + 1); } while (0)
#define N(i) (T < FS ? ((i) & 3) * 2 + ((i) >> 2) : ((i) & 3) * 2 + ((i) >> 2) ^ 2)
#define S(i) (((i) & 2) ? b : a)
#define STEP(t_) do { constexpr int T = t_, a = (T < FS ? T : 2 * T - FS) & 7, b = a + (T >= FS); constexpr bool bar = (T && !(T & 1)) || T == FS + 1; \
    if (T == ST1) STAMPT(1); if (T == ST2) STAMPT(2); if (T == ST3) STAMPT(3); if (T == ST4_) STAMPT(4); if (T == ST5) STAMPT(5); \
    bf16x8 (&C)[8] = (T & 1) ? Wb : Wa; bf16x8 (&D)[8] = (T & 1) ? Wa : Wb; \
    if (T < FS) asm volatile("s_waitcnt vmcnt(%2)" : "+v"(H[a][0]), "+v"(H[a][1]) : "n"(SC.w[T]) : "memory"); \
    else asm volatile("s_waitcnt vmcnt(%4)" : "+v"(H[a][0]), "+v"(H[a][1]), "+v"(H[b][0]), "+v"(H[b][1]) : "n"(SC.w[T]) : "memory"); \
    if (T == 0 && PIPE) { PIN(q[0]); PIN(q[1]); PIN(q[2]); PIN(q[3]); } \
    if (bar) WAIT_L0; SCHED; MM(N(0), a); if (bar) BAR; SCHED; if ((T & 3) == 3) WTOG; \
    if (T == TL) { if (wid == 0 && w < 224) { \
      SETFLAG(otid() & 63, WS_FH + (PMG(xcd) * 4 + (idx & 3)) * 1024 + 512 + (xcd >> 1) * 128 + (idx >> 2) * 8, 1); } } \
    _Pragma("unroll") for (int i = 0; i < 3; ++i) if (T == FC[i]) { PIN(f); const bool need = FMASK(i); if (!(XP & 16) && !flags_ok(f, ep, need)) spin_flags<false>(rs, fo, ep, need); } \
    if (T == FS) { lx = (otid() & 63) * 16; PIN(lx); } \
    if (T == FS + 1) { _Pragma("unroll") for (int n = 0; n < 4; ++n) t[n] = XQ(n); } \
    if (T == TY) { PIN(rv[0]); PIN(rv[1]); PIN(xv[0]); PIN(xv[1]); } \
    SCHED; \
    WRD_(NR(0)); OP(0); MM(N(1), a); WRD_(NR(1)); if (T == FS) WRX(acc[3]); RES(0); OP(1); \
    MM(N(2), b); WRD_(NR(2)); ADD4(0); RES(2); OP(2); MM(N(3), b); WRD_(NR(3)); ADD4(1); \
    OP(3); \
    MM(N(4), a); WRD_(NR(4)); WRD_(NR(5)); ADD4(2); RES(3); OP(4); if (T == 27) MM(N(6), b); MM(N(5), a); WRD_(NR(6)); WRD_(NR(7)); ADD4(3); OP(5); \
    if (T < 27) MM(N(6), b); RES(1); OP(6); MM(N(7), b); OP(7); } while (0)
  STEP(0); STEP(1); STEP(2); STEP(3); STEP(4); STEP(5); STEP(6); STEP(7); STEP(8); STEP(9); STEP(10); STEP(11); STEP(12); STEP(13);
  STEP(14); STEP(15); STEP(16); STEP(17); STEP(18); STEP(19); STAMP(4); STEP(20); STEP(21); STEP(22); STEP(23); STEP(24); STEP(25); STEP(26); STEP(27);
  STAMP(5); STAMPT(6);
  WRX(acc[1]);
  SCHED;
  auto stale = [&]() {
    asm volatile("s_waitcnt vmcnt(0)" : "+v"(pv[0]), "+v"(pv[1]), "+v"(pv[2]), "+v"(pv[3]) :: "memory");
    unsigned d[4];
#pragma unroll
    for (int n = 0; n < 4; ++n) d[n] = __builtin_amdgcn_bitop3_b32(pv[n][1], pv[n][3], tag, 0x7e);
    return __builtin_amdgcn_ballot_w64((__builtin_amdgcn_bitop3_b32(d[0], d[1], d[2], 0xfe) | d[3]) != 0) != 0;
  };
  if (__builtin_expect(stale(), 0)) {
#pragma unroll 1
    for (unsigned spins = 0; spins < SPIN_CAP; ++spins) { LDP(0); LDP(1); LDP(2); LDP(3); if (!stale()) break; }
  }
  WAIT_L0; BAR; SCHED;
#pragma unroll
  for (int n = 0; n < 4; ++n) t[n] = XQ(n);
  SCHED;
  f32x4 pa[4];
#pragma unroll
  for (int n = 0; n < 4; ++n) { pa[n] = acc[0][n] + (f32x4){bf_lo(pv[n][0]), bf_hi(pv[n][0]), bf_lo(pv[n][2]), bf_hi(pv[n][2])}; PIN(pa[n]); }
  SCHED;
  STAMP(6);
  const __amdgpu_buffer_rsrc_t rO = MKRSRC(out, S_ * D_ * 2);
#pragma unroll
  for (int i = 0; i < 2; ++i) {
    const f32x4 a = t[i] + pa[i], b = t[2 + i] + pa[2 + i];
    const u32x2 p = __builtin_amdgcn_permlane16_swap(pack2(a[0], a[1]), pack2(b[0], b[1]), false, false), q = __builtin_amdgcn_permlane16_swap(pack2(a[2], a[3]), pack2(b[2], b[3]), false, false);
    const u32x4 bv = {p[0], q[0], p[1], q[1]};
    u32x4 o;
#pragma unroll
    for (int n = 0; n < 4; ++n) o[n] = pack2(rf[8 * i + 2 * n] + bf_lo(bv[n]), rf[8 * i + 2 * n + 1] + bf_hi(bv[n]));
    __builtin_amdgcn_raw_buffer_store_b128(o, rO, ro + i * 32768, 0, OAUX);
  }
}

__global__ __launch_bounds__(512, 2) void k_main(const u16* __restrict__ x, const u16* __restrict__ w_r, const u16* __restrict__ w_i, const u16* __restrict__ wg,
                                                 const u16* __restrict__ wu, const u16* __restrict__ wd, unsigned char* __restrict__ ws, u16* __restrict__ out) {
  extern __shared__ __attribute__((aligned(16))) unsigned char smem_[];
  LAS unsigned char* lds = (LAS unsigned char*)smem_;
  STAMP_DECL; STAMP(0);
  unsigned pf = 0;
  unsigned t = threadIdx.x; int w = blockIdx.x;
  if (PFC) asm volatile("s_getpc_b64 s[92:93]\n.Lcode0:\n\ts_lshl_b32 s95, %2, 4\n\ts_mov_b32 s94, .Lcode_end-.Lcode0\n\tv_lshl_add_u32 %0, %1, 12, s95\n\ts_and_b32 s93, s93, 0xffff\n\ts_mov_b32 s95, 0x20000\n\t"
                        "buffer_load_dword %0, %0, s[92:95], 0 offen" : "=&v"(pf), "+v"(t), "+s"(w) :: "s92", "s93", "s94", "s95", "scc", "memory");
  {
    const int j = __builtin_amdgcn_readfirstlane(t >> 6);
    const unsigned vo = (t & 63) * 16, lb = (unsigned)(size_t)lds + j * 2048, lx = lb + (w < 224 ? K1_SCR : 65536), so = ROW(w, j) * 2048;
    const __amdgpu_buffer_rsrc_t rT = MKRSRC(&g_tab, sizeof(Tabs));
    DMA16(rT, vo, j * 2048, lb, L_TROW); DMA16(rT, vo, j * 2048 + 1024, lb, L_TROW + 1024);
    asm volatile("" :: "s"(lx), "s"(so));
    SCHED;
    const __amdgpu_buffer_rsrc_t rX = MKRSRC(x, S_ * D_ * 2);
    DMA16P(rX, vo, so, lx, 0, POLX); DMA16P(rX, vo, so + 1024, lx, 1024, POLX);
    SCHED;
  }
  asm volatile("" :: "s"(x), "s"(w_r), "s"(w_i), "s"(wg), "s"(wu), "s"(wd), "s"(ws), "s"(out));
  const __amdgpu_buffer_rsrc_t rs = __builtin_amdgcn_make_buffer_rsrc((void*)ws, 0, WS_USED, 0x00020000);
  unsigned long long ep;
  Rest R;
#define FLIP 0
  unsigned role = FLIP ? (blockIdx.x - 224) >> 31 : blockIdx.x < 224;
  if (FLIP) asm volatile("" : "+s"(role));
  if (!role) {
    ep = fft_group(lds, x, w_r, w_i, wd, ws, rs, R, pf STAMP_PASS);
  } else {
    ep = gemm1_group(lds, x, w_r, w_i, wg, wu, wd, ws, rs, R, pf STAMP_PASS);
  }
  STAMP_FLUSH(blockIdx.x >= 224);
  STAMP_RESET; STAMP(0);
  phase2(lds, x, wd, ws, out, rs, ep, R STAMP_PASS);
  STAMP_FLUSH(4);
  asm volatile(".Lcode_end:");
}

extern "C" void kernel_launch(void* const* d_in, const int* in_sizes, int n_in, void* d_out, int out_size, void* d_ws, size_t ws_size, hipStream_t stream) {
  const u16* x = (const u16*)d_in[0]; const u16* w_r = (const u16*)d_in[1]; const u16* w_i = (const u16*)d_in[2];
  const u16* wg = (const u16*)d_in[4]; const u16* wu = (const u16*)d_in[5]; const u16* wd = (const u16*)d_in[6];
  (void)hipFuncSetAttribute((const void*)k_main, hipFuncAttributeMaxDynamicSharedMemorySize, K1_LDS);
  k_main<<<256, 512, K1_LDS, stream>>>(x, w_r, w_i, wg, wu, wd, (unsigned char*)d_ws, (u16*)d_out);
}
```
